```python
import torch

def _softmax(x, axis=-1):
    return torch.softmax(x, dim=axis)
def _gelu(x):
    return torch.nn.functional.gelu(x, approximate="tanh")
def _silu(x):
    return torch.nn.functional.silu(x)
def _sigmoid(x):
    return torch.sigmoid(x)
def _layernorm(x, axis=-1, eps=1e-5):
    m = x.mean(dim=axis, keepdim=True)
    v = x.var(dim=axis, keepdim=True, unbiased=False)
    return (x - m) / torch.sqrt(v + eps)
def _scatter_add(tgt, idx, src, axis=0):
    return tgt.clone().index_add_(axis, idx, src)
def _segment_sum(src, seg, n):
    out = torch.zeros((n,) + src.shape[1:], dtype=src.dtype)
    return out.index_add_(0, seg, src)
def _pad(x, pw):
    flat = tuple(v for pair in reversed(pw) for v in pair)
    return torch.nn.functional.pad(x, flat)

kernel_name = 'parallel_sum__grouped_kv_mqa__dense_activation_gelu__prompt'

def setup_inputs():

    _w = (lambda a: (lambda t: t.to(torch.bfloat16) if t.is_floating_point() else t)(torch.as_tensor(a)))
    def _A_setup():
        B, S, D, H = 1, 2048, 512, 32
        Dh = D // H
        return {
            "x": _w((lambda *s: torch.randn(*s, dtype=torch.bfloat16) * 0.05)(B, S, D)),
            "wq": _w((lambda *s: torch.randn(*s, dtype=torch.bfloat16) * 0.05)(D, H * Dh)),
            "wk": _w((lambda *s: torch.randn(*s, dtype=torch.bfloat16) * 0.05)(D, Dh)),
            "wv": _w((lambda *s: torch.randn(*s, dtype=torch.bfloat16) * 0.05)(D, Dh)),
            "wo": _w((lambda *s: torch.randn(*s, dtype=torch.bfloat16) * 0.05)(H * Dh, D)),
        }
    _A_in = _A_setup()

    def _B_setup():
        B, S, D, F = 1, 2048, 512, 1376
        return {
            "x": _w((lambda *s: torch.randn(*s, dtype=torch.bfloat16) * 0.05)(B, S, D)),
            "w1": _w((lambda *s: torch.randn(*s, dtype=torch.bfloat16) * 0.8)(D, F) * (D ** -0.5)),
            "b1": _w((lambda *s: torch.randn(*s, dtype=torch.bfloat16) * 12.8)(F) * 0.01),
            "w2": _w((lambda *s: torch.randn(*s, dtype=torch.bfloat16) * 0.8)(F, D) * (F ** -0.5)),
            "b2": _w((lambda *s: torch.randn(*s, dtype=torch.bfloat16) * 12.8)(D) * 0.01),
        }
    _B_in = _B_setup()

    return {
        "A_x": _A_in["x"],
        "A_wq": _A_in["wq"],
        "A_wk": _A_in["wk"],
        "A_wv": _A_in["wv"],
        "A_wo": _A_in["wo"],
        "B_x": _B_in["x"],
        "B_w1": _B_in["w1"],
        "B_b1": _B_in["b1"],
        "B_w2": _B_in["w2"],
        "B_b2": _B_in["b2"],
    }

def reference(A_x, A_wq, A_wk, A_wv, A_wo, B_x, B_w1, B_b1, B_w2, B_b2):
    def _A_ref(x, wq, wk, wv, wo):
        B, S, D = x.shape
        H = 32
        Dh = D // H
        q = (lambda x, *d: x.permute(*d))((x @ wq).reshape(B, S, H, Dh), 0, 2, 1, 3)
        k = x @ wk
        v = x @ wv
        scores = torch.einsum("bhqd,bkd->bhqk", q, k) * (Dh ** -0.5)
        p = _softmax(scores, axis=-1)
        o = torch.einsum("bhqk,bkd->bhqd", p, v)
        return (lambda x, *d: x.permute(*d))(o, 0, 2, 1, 3).reshape(B, S, H * Dh) @ wo

    def _B_ref(x, w1, b1, w2, b2):
        h = torch.einsum("bsd,df->bsf", x, w1) + b1
        h = _gelu(h)
        return torch.einsum("bsf,fd->bsd", h, w2) + b2

    x = A_x
    xn = _layernorm(x, axis=-1)
    return x + _A_ref(xn, A_wq, A_wk, A_wv, A_wo) + _B_ref(xn, B_w1, B_b1, B_w2, B_b2)
```

```cpp
#include <hip/hip_runtime.h>
#include <hip/hip_bf16.h>
typedef __hip_bfloat16 bf16;
constexpr int S=2048, D=512, H=32, DH=16, F=1376;
__device__ inline float b2f(bf16 x){return __bfloat162float(x);}
__global__ void ln_k(const bf16* x, float* xn){
    int r=blockIdx.x; __shared__ float sh[2];
    float v[2]; float s=0;
    for(int i=0;i<2;i++){v[i]=b2f(x[r*D+threadIdx.x+256*i]); s+=v[i];}
    __shared__ float red[256];
    red[threadIdx.x]=s; __syncthreads();
    for(int o=128;o>0;o>>=1){ if(threadIdx.x<o) red[threadIdx.x]+=red[threadIdx.x+o]; __syncthreads();}
    float m=red[0]/D; __syncthreads();
    s=0; for(int i=0;i<2;i++){ s+=(v[i]-m)*(v[i]-m);} red[threadIdx.x]=s; __syncthreads();
    for(int o=128;o>0;o>>=1){ if(threadIdx.x<o) red[threadIdx.x]+=red[threadIdx.x+o]; __syncthreads();}
    float rs=rsqrtf(red[0]/D+1e-5f);
    for(int i=0;i<2;i++) xn[r*D+threadIdx.x+256*i]=b2f(__float2bfloat16((v[i]-m)*rs));
}
__global__ void gemm_k(const float* A, const bf16* W, const bf16* bias, float* C, int M,int N,int K,int mode){
    int n=blockIdx.x*64+threadIdx.x%64; int m=blockIdx.y*4+threadIdx.x/64;
    if(n>=N) return;
    float acc=0; for(int k=0;k<K;k++) acc+=A[m*K+k]*b2f(W[k*N+n]);
    if(mode==1){ acc+=b2f(bias[n]); float u=0.7978845608f*(acc+0.044715f*acc*acc*acc); acc=0.5f*acc*(1.f+tanhf(u)); }
    if(mode==2){ acc+=b2f(bias[n]); }
    C[m*N+n]=b2f(__float2bfloat16(acc));
}
__global__ void attn_k(const float* q,const float* k,const float* v,float* o){
    int row=blockIdx.x*256+threadIdx.x; int t=row/H, h=row%H;
    float qq[DH]; for(int d=0;d<DH;d++) qq[d]=q[t*D+h*DH+d]*0.25f;
    float m=-1e30f,l=0; float acc[DH]; for(int d=0;d<DH;d++) acc[d]=0;
    for(int j=0;j<S;j++){ float s=0; for(int d=0;d<DH;d++) s+=qq[d]*k[j*DH+d];
        float mn=fmaxf(m,s); float a=__expf(m-mn), p=__expf(s-mn); l=l*a+p; for(int d=0;d<DH;d++) acc[d]=acc[d]*a+p*v[j*DH+d]; m=mn; }
    for(int d=0;d<DH;d++) o[t*D+h*DH+d]=b2f(__float2bfloat16(acc[d]/l));
}
__global__ void fin_k(const bf16* x,const float* a,const float* b,bf16* out){
    int i=blockIdx.x*256+threadIdx.x; out[i]=__float2bfloat16(b2f(x[i])+a[i]+b[i]);
}
extern "C" void kernel_launch(void* const* d_in, const int* in_sizes, int n_in, void* d_out, int out_size, void* d_ws, size_t ws_size, hipStream_t stream) {
    const bf16* x=(const bf16*)d_in[0]; const bf16* wq=(const bf16*)d_in[1]; const bf16* wk=(const bf16*)d_in[2]; const bf16* wv=(const bf16*)d_in[3]; const bf16* wo=(const bf16*)d_in[4];
    const bf16* w1=(const bf16*)d_in[6]; const bf16* b1=(const bf16*)d_in[7]; const bf16* w2=(const bf16*)d_in[8]; const bf16* b2=(const bf16*)d_in[9];
    float* ws=(float*)d_ws; float* xn=ws; float* q=xn+S*D; float* k=q+S*D; float* v=k+S*DH; float* o=v+S*DH; float* h=o+S*D; float* a=h+S*F; float* b=a+S*D;
    ln_k<<<S,256,0,stream>>>(x,xn);
    gemm_k<<<dim3(D/64,S/4),256,0,stream>>>(xn,wq,nullptr,q,S,D,D,0);
    gemm_k<<<dim3(1,S/4),256,0,stream>>>(xn,wk,nullptr,k,S,DH,D,0);
    gemm_k<<<dim3(1,S/4),256,0,stream>>>(xn,wv,nullptr,v,S,DH,D,0);
    attn_k<<<S*H/256,256,0,stream>>>(q,k,v,o);
    gemm_k<<<dim3(D/64,S/4),256,0,stream>>>(o,wo,nullptr,a,S,D,D,0);
    gemm_k<<<dim3((F+63)/64,S/4),256,0,stream>>>(xn,w1,b1,h,S,F,D,1);
    gemm_k<<<dim3(D/64,S/4),256,0,stream>>>(h,w2,b2,b,S,D,F,2);
    fin_k<<<S*D/256,256,0,stream>>>(x,a,b,(bf16*)d_out);
}
```

```cpp
#include <hip/hip_runtime.h>
#include <hip/hip_bf16.h>
#include <stdint.h>
#include <utility>

typedef __attribute__((ext_vector_type(16))) float f32x16;
typedef __attribute__((ext_vector_type(8))) float f32x8;
typedef __attribute__((ext_vector_type(4))) float f32x4;
typedef __attribute__((ext_vector_type(2))) float f32x2;
typedef __attribute__((ext_vector_type(8))) __bf16 bf16x8;
typedef __attribute__((ext_vector_type(16))) __bf16 bf16x16;
typedef __attribute__((ext_vector_type(2))) __bf16 bf16x2;
typedef __attribute__((ext_vector_type(4))) unsigned u32x4;
typedef __attribute__((ext_vector_type(2))) unsigned u32x2;
typedef __attribute__((ext_vector_type(4))) short s16x4;
#define LAS __attribute__((address_space(3)))
#define DI __device__ __forceinline__

constexpr unsigned WS_EPOCH = 0, WS_KVF = 4096, WS_HF = 8192, WS_OF = 16384, WS_K = 65536, WS_V = 131072, WS_H = 262144,
                   WS_O = WS_H + 32 * 2 * 86 * 1024, WS_BYTES = WS_O + 32 * 2 * 32 * 1024;
constexpr unsigned WS_DBG = 16u << 20;
constexpr unsigned VS = 544, VT = 2 * VS, SLOT = 8192 + 8 * VT;
constexpr unsigned L_WKV = SLOT, L_RING = 2 * SLOT, RING = 6, L_X = L_RING + 8 * RING * 1024, XP = 1040;
constexpr unsigned WSL = 24576, L_W = 3 * SLOT - WSL;
constexpr unsigned L_Q = L_X + 64 * XP, L_ST = L_Q + 8192, L_MISC = L_ST + 4096, LDS_BYTES = 160 * 1024;
static_assert(L_W + 5 * WSL <= L_Q && L_MISC + 256 <= LDS_BYTES, "lds map");
constexpr unsigned SPIN_CAP = 1u << 18;
constexpr float QSCALE = 0.25f * 1.4426950408889634f;
constexpr float SAFE_LOG2 = 60.f;

template <class F, int... I> DI void static_for_(F&& f, std::integer_sequence<int, I...>) { (f(std::integral_constant<int, I>{}), ...); }
template <int N, class F> DI void static_for(F&& f) { static_for_(f, std::make_integer_sequence<int, N>{}); }

DI unsigned cvtpk(float lo, float hi) { bf16x2 v = {(__bf16)lo, (__bf16)hi}; return __builtin_bit_cast(unsigned, v); }
DI float bfr(float x) { return __uint_as_float(cvtpk(x, x) & 0xFFFF0000u); }
DI float dot2(unsigned a, unsigned b, float c) { return __builtin_amdgcn_fdot2_f32_bf16(__builtin_bit_cast(bf16x2, a), __builtin_bit_cast(bf16x2, b), c, false); }
DI u32x4 lr128(unsigned a) { return *(const LAS u32x4*)(size_t)a; }
DI u32x2 lr64(unsigned a) { return *(const LAS u32x2*)(size_t)a; }
DI void lw128(unsigned a, u32x4 v) { *(LAS u32x4*)(size_t)a = v; }
DI void lw64(unsigned a, u32x2 v) { *(LAS u32x2*)(size_t)a = v; }
DI void lw32(unsigned a, unsigned v) { *(LAS unsigned*)(size_t)a = v; }
DI unsigned lr32(unsigned a) { return *(const LAS unsigned*)(size_t)a; }
DI u32x2 trr(unsigned a) { return __builtin_bit_cast(u32x2, __builtin_amdgcn_ds_read_tr16_b64_v4i16((LAS s16x4*)(size_t)a)); }
DI u32x4 trr2(unsigned a, unsigned d) { u32x2 l = trr(a), h = trr(a + d); return (u32x4){l[0], l[1], h[0], h[1]}; }
DI f32x16 mfma32(u32x4 a, u32x4 b, f32x16 c) { return __builtin_amdgcn_mfma_f32_32x32x16_bf16(__builtin_bit_cast(bf16x8, a), __builtin_bit_cast(bf16x8, b), c, 0, 0, 0); }
DI f32x4 mfma16(u32x4 a, u32x4 b, f32x4 c) { return __builtin_amdgcn_mfma_f32_16x16x32_bf16(__builtin_bit_cast(bf16x8, a), __builtin_bit_cast(bf16x8, b), c, 0, 0, 0); }
DI f32x16 smf(u32x4 a, f32x8 b, f32x16 c, unsigned idx) { return __builtin_amdgcn_smfmac_f32_32x32x32_bf16(__builtin_bit_cast(bf16x8, a), __builtin_bit_cast(bf16x16, b), c, idx, 0, 0); }
DI void glds(unsigned voff, const void* sbase, unsigned dst) {
    asm volatile("s_mov_b32 m0, %2\n\ts_nop 0\n\tglobal_load_lds_dwordx4 %0, %1" ::"v"(voff), "s"(sbase), "s"(dst) : "memory");
}
template <int N> DI void waitv() { asm volatile("s_waitcnt vmcnt(%0)" ::"n"(N) : "memory"); }
template <class T> DI void PIN(T& x) { asm volatile("" : "+v"(x)); }
template <class T> DI void USE(const T& x) { asm volatile("" ::"v"(x) : "memory"); }
DI void CFENCE() { asm volatile("" ::: "memory"); }
DI void LBAR() { asm volatile("s_waitcnt lgkmcnt(0)\n\ts_barrier" ::: "memory"); }
DI float sw32(float x, float& o) { auto s = __builtin_amdgcn_permlane32_swap(__float_as_uint(x), __float_as_uint(x), false, false); o = __uint_as_float(s[1]); return __uint_as_float(s[0]); }
DI float sw16(float x, float& o) { auto s = __builtin_amdgcn_permlane16_swap(__float_as_uint(x), __float_as_uint(x), false, false); o = __uint_as_float(s[1]); return __uint_as_float(s[0]); }
DI float add32(float x) { float o, a = sw32(x, o); return a + o; }
DI float add16(float x) { float o, a = sw16(x, o); return a + o; }
DI float max32(float x) { float o, a = sw32(x, o); return fmaxf(a, o); }
DI float max16(float x) { float o, a = sw16(x, o); return fmaxf(a, o); }
template <int C> DI float dpp(float x) { return __uint_as_float(__builtin_amdgcn_update_dpp(0, __float_as_uint(x), C, 0xF, 0xF, false)); }
DI float rowmax16(float x) { x = fmaxf(x, dpp<0x128>(x)); x = fmaxf(x, dpp<0x124>(x)); x = fmaxf(x, dpp<0x122>(x)); return fmaxf(x, dpp<0x121>(x)); }
DI float wavemax(float x) { return max32(max16(rowmax16(x))); }
DI void ln_stats(unsigned a, float& mu, float& sg, float& rs) {
    float s1 = 0.f, s2 = 0.f;
#pragma unroll
    for (int i = 0; i < 4; ++i) { u32x4 v = lr128(a + i * 16); s1 += __uint_as_float(v[0]) + __uint_as_float(v[2]); s2 += __uint_as_float(v[1]) + __uint_as_float(v[3]); }
    mu = s1 * (1.f / 512.f);
    const float var = fmaxf(s2 * (1.f / 512.f) - mu * mu, 0.f) + 1e-5f;
    rs = __builtin_amdgcn_rsqf(var); sg = var * rs;
}
DI u32x4 ext_c(float c) { const float a = bfr(c), r = c - a, b = bfr(r), d = r - b; return (u32x4){cvtpk(a, a), cvtpk(b, a), cvtpk(b, d), 0u}; }
DI u32x4 ext_m(float m) { const float a = bfr(m), r = m - a, b = bfr(r), d = r - b; return (u32x4){cvtpk(-a, -b), cvtpk(-a, -d), cvtpk(-b, -a), 0u}; }
DI u32x4 ext_s(float s) { const float a = bfr(s), r = s - a, b = bfr(r), d = r - b; return (u32x4){cvtpk(a, b), cvtpk(d, 0.f), 0u, 0u}; }
DI void lds_wait_cnt(unsigned a, unsigned n) {
    for (unsigned it = 0; it < SPIN_CAP; ++it) {
        unsigned v; asm volatile("ds_read_b32 %0, %1\n\ts_waitcnt lgkmcnt(0)" : "=v"(v) : "v"(a) : "memory");
        if (__builtin_amdgcn_readfirstlane(v) >= n) break;
    }
}
#define LD128(off) __builtin_amdgcn_raw_buffer_load_b128(rs, (off), 0, 16)
#define LD32(off) __builtin_amdgcn_raw_buffer_load_b32(rs, (off), 0, 16)
#define ST128(v, off) __builtin_amdgcn_raw_buffer_store_b128((v), rs, (off), 0, 16)
#define ST64(v, off) __builtin_amdgcn_raw_buffer_store_b64((v), rs, (off), 0, 16)
#define ST32(v, off) __builtin_amdgcn_raw_buffer_store_b32((v), rs, (off), 0, 16)

#define STAMP(i)

#define ATT_STAGE_F \
    "s_setprio 3\n" \
    "v_exp_f32 v96, v96\n" \
    "v_exp_f32 v97, v97\n" \
    "v_smfmac_f32_32x32x32_bf16 v[80:95], v[156:159], v[120:127], %[idx]\n" \
    "ds_read_b128 v[140:143], %[ka] offset:1024\n" \
    "ds_read_b128 v[152:155], %[va] offset:1088\n" \
    "ds_read_b128 v[156:159], %[va] offset:1632\n" \
    "v_exp_f32 v98, v98\n" \
    "v_exp_f32 v99, v99\n" \
    "v_exp_f32 v100, v100\n" \
    "v_mfma_f32_32x32x16_bf16 v[112:127], v[136:139], v[132:135], 0\n" \
    "v_exp_f32 v101, v101\n" \
    "v_exp_f32 v102, v102\n" \
    "v_exp_f32 v103, v103\n" \
    "v_exp_f32 v104, v104\n" \
    "v_exp_f32 v105, v105\n" \
    "v_smfmac_f32_32x32x32_bf16 v[64:79], v[144:147], v[96:103], %[idx]\n" \
    "v_exp_f32 v106, v106\n" \
    "v_exp_f32 v107, v107\n" \
    "v_exp_f32 v108, v108\n" \
    "v_exp_f32 v109, v109\n" \
    "v_exp_f32 v110, v110\n" \
    "v_exp_f32 v111, v111\n" \
    "v_exp_f32 v112, v112\n" \
    "v_exp_f32 v113, v113\n" \
    "v_smfmac_f32_32x32x32_bf16 v[64:79], v[148:151], v[104:111], %[idx]\n" \
    "v_exp_f32 v114, v114\n" \
    "v_exp_f32 v115, v115\n" \
    "v_exp_f32 v116, v116\n" \
    "s_waitcnt lgkmcnt(0)\n" \
    "v_mfma_f32_32x32x16_bf16 v[96:111], v[140:143], v[128:131], 0\n" \
    "ds_write_b128 %[kw], %[kst]\n" \
    "v_exp_f32 v117, v117\n" \
    "v_exp_f32 v118, v118\n" \
    "v_exp_f32 v119, v119\n" \
    "v_exp_f32 v120, v120\n" \
    "v_exp_f32 v121, v121\n" \
    "v_smfmac_f32_32x32x32_bf16 v[80:95], v[144:147], v[112:119], %[idx]\n" \
    "v_exp_f32 v122, v122\n" \
    "v_exp_f32 v123, v123\n" \
    "v_exp_f32 v124, v124\n" \
    "v_exp_f32 v125, v125\n" \
    "v_exp_f32 v126, v126\n" \
    "v_exp_f32 v127, v127\n" \
    "s_setprio 2\n" \
    "v_exp_f32 v96, v96\n" \
    "v_exp_f32 v97, v97\n" \
    "v_smfmac_f32_32x32x32_bf16 v[80:95], v[148:151], v[120:127], %[idx]\n" \
    "ds_read_b128 v[136:139], %[ka] offset:2048\n" \
    "ds_read_b128 v[144:147], %[va] offset:2176\n" \
    "ds_read_b128 v[148:151], %[va] offset:2720\n" \
    "v_exp_f32 v98, v98\n" \
    "v_exp_f32 v99, v99\n" \
    "v_exp_f32 v100, v100\n" \
    "v_mfma_f32_32x32x16_bf16 v[112:127], v[140:143], v[132:135], 0\n" \
    "v_exp_f32 v101, v101\n" \
    "v_exp_f32 v102, v102\n" \
    "v_exp_f32 v103, v103\n" \
    "v_exp_f32 v104, v104\n" \
    "v_exp_f32 v105, v105\n" \
    "v_smfmac_f32_32x32x32_bf16 v[64:79], v[152:155], v[96:103], %[idx]\n" \
    "v_exp_f32 v106, v106\n" \
    "v_exp_f32 v107, v107\n" \
    "v_exp_f32 v108, v108\n" \
    "v_exp_f32 v109, v109\n" \
    "v_exp_f32 v110, v110\n" \
    "v_exp_f32 v111, v111\n" \
    "v_exp_f32 v112, v112\n" \
    "v_exp_f32 v113, v113\n" \
    "v_smfmac_f32_32x32x32_bf16 v[64:79], v[156:159], v[104:111], %[idx]\n" \
    "v_exp_f32 v114, v114\n" \
    "v_exp_f32 v115, v115\n" \
    "v_exp_f32 v116, v116\n" \
    "s_waitcnt lgkmcnt(0)\n" \
    "v_mfma_f32_32x32x16_bf16 v[96:111], v[136:139], v[128:131], 0\n" \
    "ds_write_b128 %[vw], %[vst]\n" \
    "v_exp_f32 v117, v117\n" \
    "v_exp_f32 v118, v118\n" \
    "v_exp_f32 v119, v119\n" \
    "v_exp_f32 v120, v120\n" \
    "v_exp_f32 v121, v121\n" \
    "v_smfmac_f32_32x32x32_bf16 v[80:95], v[152:155], v[112:119], %[idx]\n" \
    "v_exp_f32 v122, v122\n" \
    "v_exp_f32 v123, v123\n" \
    "v_exp_f32 v124, v124\n" \
    "v_exp_f32 v125, v125\n" \
    "v_exp_f32 v126, v126\n" \
    "v_exp_f32 v127, v127\n" \
    "s_setprio 1\n" \
    "v_exp_f32 v96, v96\n" \
    "v_exp_f32 v97, v97\n" \
    "v_smfmac_f32_32x32x32_bf16 v[80:95], v[156:159], v[120:127], %[idx]\n" \
    "ds_read_b128 v[140:143], %[ka] offset:3072\n" \
    "ds_read_b128 v[152:155], %[va] offset:3264\n" \
    "ds_read_b128 v[156:159], %[va] offset:3808\n" \
    "v_exp_f32 v98, v98\n" \
    "v_exp_f32 v99, v99\n" \
    "v_exp_f32 v100, v100\n" \
    "v_mfma_f32_32x32x16_bf16 v[112:127], v[136:139], v[132:135], 0\n" \
    "v_exp_f32 v101, v101\n" \
    "v_exp_f32 v102, v102\n" \
    "v_exp_f32 v103, v103\n" \
    "v_exp_f32 v104, v104\n" \
    "v_exp_f32 v105, v105\n" \
    "v_smfmac_f32_32x32x32_bf16 v[64:79], v[144:147], v[96:103], %[idx]\n" \
    "v_exp_f32 v106, v106\n" \
    "v_exp_f32 v107, v107\n" \
    "v_exp_f32 v108, v108\n" \
    "v_exp_f32 v109, v109\n" \
    "v_exp_f32 v110, v110\n" \
    "v_exp_f32 v111, v111\n" \
    "v_exp_f32 v112, v112\n" \
    "v_exp_f32 v113, v113\n" \
    "v_smfmac_f32_32x32x32_bf16 v[64:79], v[148:151], v[104:111], %[idx]\n" \
    "v_exp_f32 v114, v114\n" \
    "v_exp_f32 v115, v115\n" \
    "v_exp_f32 v116, v116\n" \
    "s_waitcnt lgkmcnt(0)\n" \
    "v_mfma_f32_32x32x16_bf16 v[96:111], v[140:143], v[128:131], 0\n" \
    "v_exp_f32 v117, v117\n" \
    "v_exp_f32 v118, v118\n" \
    "v_exp_f32 v119, v119\n" \
    "v_exp_f32 v120, v120\n" \
    "v_exp_f32 v121, v121\n" \
    "v_smfmac_f32_32x32x32_bf16 v[80:95], v[144:147], v[112:119], %[idx]\n" \
    "v_exp_f32 v122, v122\n" \
    "v_exp_f32 v123, v123\n" \
    "v_exp_f32 v124, v124\n" \
    "v_exp_f32 v125, v125\n" \
    "v_exp_f32 v126, v126\n" \
    "v_exp_f32 v127, v127\n" \
    "s_barrier\n" \
    "s_setprio 0\n" \
    "v_exp_f32 v96, v96\n" \
    "v_exp_f32 v97, v97\n" \
    "v_smfmac_f32_32x32x32_bf16 v[80:95], v[148:151], v[120:127], %[idx]\n" \
    "ds_read_b128 v[136:139], %[kan]\n" \
    "ds_read_b128 v[144:147], %[van]\n" \
    "ds_read_b128 v[148:151], %[van] offset:544\n" \
    "v_exp_f32 v98, v98\n" \
    "v_exp_f32 v99, v99\n" \
    "v_exp_f32 v100, v100\n" \
    "v_mfma_f32_32x32x16_bf16 v[112:127], v[140:143], v[132:135], 0\n" \
    "v_exp_f32 v101, v101\n" \
    "v_exp_f32 v102, v102\n" \
    "v_exp_f32 v103, v103\n" \
    "v_exp_f32 v104, v104\n" \
    "v_exp_f32 v105, v105\n" \
    "v_smfmac_f32_32x32x32_bf16 v[64:79], v[152:155], v[96:103], %[idx]\n" \
    "v_exp_f32 v106, v106\n" \
    "v_exp_f32 v107, v107\n" \
    "v_exp_f32 v108, v108\n" \
    "v_exp_f32 v109, v109\n" \
    "v_exp_f32 v110, v110\n" \
    "v_exp_f32 v111, v111\n" \
    "v_exp_f32 v112, v112\n" \
    "v_exp_f32 v113, v113\n" \
    "v_smfmac_f32_32x32x32_bf16 v[64:79], v[156:159], v[104:111], %[idx]\n" \
    "v_exp_f32 v114, v114\n" \
    "v_exp_f32 v115, v115\n" \
    "v_exp_f32 v116, v116\n" \
    "s_waitcnt lgkmcnt(0)\n" \
    "v_mfma_f32_32x32x16_bf16 v[96:111], v[136:139], v[128:131], 0\n" \
    "v_exp_f32 v117, v117\n" \
    "v_exp_f32 v118, v118\n" \
    "v_exp_f32 v119, v119\n" \
    "v_exp_f32 v120, v120\n" \
    "v_exp_f32 v121, v121\n" \
    "v_smfmac_f32_32x32x32_bf16 v[80:95], v[152:155], v[112:119], %[idx]\n" \
    "v_exp_f32 v122, v122\n" \
    "v_exp_f32 v123, v123\n" \
    "v_exp_f32 v124, v124\n" \
    "v_exp_f32 v125, v125\n" \
    "v_exp_f32 v126, v126\n" \
    "v_exp_f32 v127, v127\n" \
    "s_barrier\n"
#define ATT_STAGE_W \
    "s_setprio 3\n" \
    "v_exp_f32 v96, v96\n" \
    "v_exp_f32 v97, v97\n" \
    "v_smfmac_f32_32x32x32_bf16 v[80:95], v[156:159], v[120:127], %[idx]\n" \
    "ds_read_b128 v[140:143], %[ka] offset:1024\n" \
    "ds_read_b128 v[152:155], %[va] offset:1088\n" \
    "ds_read_b128 v[156:159], %[va] offset:1632\n" \
    "v_exp_f32 v98, v98\n" \
    "v_exp_f32 v99, v99\n" \
    "v_exp_f32 v100, v100\n" \
    "v_mfma_f32_32x32x16_bf16 v[112:127], v[136:139], v[132:135], 0\n" \
    "v_exp_f32 v101, v101\n" \
    "v_exp_f32 v102, v102\n" \
    "v_exp_f32 v103, v103\n" \
    "v_exp_f32 v104, v104\n" \
    "v_exp_f32 v105, v105\n" \
    "v_smfmac_f32_32x32x32_bf16 v[64:79], v[144:147], v[96:103], %[idx]\n" \
    "v_exp_f32 v106, v106\n" \
    "v_exp_f32 v107, v107\n" \
    "v_exp_f32 v108, v108\n" \
    "v_exp_f32 v109, v109\n" \
    "v_exp_f32 v110, v110\n" \
    "v_exp_f32 v111, v111\n" \
    "v_exp_f32 v112, v112\n" \
    "v_exp_f32 v113, v113\n" \
    "v_smfmac_f32_32x32x32_bf16 v[64:79], v[148:151], v[104:111], %[idx]\n" \
    "v_exp_f32 v114, v114\n" \
    "v_exp_f32 v115, v115\n" \
    "v_exp_f32 v116, v116\n" \
    "s_waitcnt lgkmcnt(0)\n" \
    "v_mfma_f32_32x32x16_bf16 v[96:111], v[140:143], v[128:131], 0\n" \
    "ds_write_b128 %[kw], %[kst]\n" \
    "v_exp_f32 v117, v117\n" \
    "v_exp_f32 v118, v118\n" \
    "v_exp_f32 v119, v119\n" \
    "v_exp_f32 v120, v120\n" \
    "v_exp_f32 v121, v121\n" \
    "v_smfmac_f32_32x32x32_bf16 v[80:95], v[144:147], v[112:119], %[idx]\n" \
    "v_exp_f32 v122, v122\n" \
    "v_exp_f32 v123, v123\n" \
    "v_exp_f32 v124, v124\n" \
    "v_exp_f32 v125, v125\n" \
    "v_exp_f32 v126, v126\n" \
    "v_exp_f32 v127, v127\n" \
    "s_setprio 2\n" \
    "v_exp_f32 v96, v96\n" \
    "v_exp_f32 v97, v97\n" \
    "v_smfmac_f32_32x32x32_bf16 v[80:95], v[148:151], v[120:127], %[idx]\n" \
    "ds_read_b128 v[136:139], %[ka] offset:2048\n" \
    "ds_read_b128 v[144:147], %[va] offset:2176\n" \
    "ds_read_b128 v[148:151], %[va] offset:2720\n" \
    "v_exp_f32 v98, v98\n" \
    "v_exp_f32 v99, v99\n" \
    "v_exp_f32 v100, v100\n" \
    "v_mfma_f32_32x32x16_bf16 v[112:127], v[140:143], v[132:135], 0\n" \
    "v_exp_f32 v101, v101\n" \
    "v_exp_f32 v102, v102\n" \
    "v_exp_f32 v103, v103\n" \
    "v_exp_f32 v104, v104\n" \
    "v_exp_f32 v105, v105\n" \
    "v_smfmac_f32_32x32x32_bf16 v[64:79], v[152:155], v[96:103], %[idx]\n" \
    "v_exp_f32 v106, v106\n" \
    "v_exp_f32 v107, v107\n" \
    "v_exp_f32 v108, v108\n" \
    "v_exp_f32 v109, v109\n" \
    "v_exp_f32 v110, v110\n" \
    "v_exp_f32 v111, v111\n" \
    "v_exp_f32 v112, v112\n" \
    "v_exp_f32 v113, v113\n" \
    "v_smfmac_f32_32x32x32_bf16 v[64:79], v[156:159], v[104:111], %[idx]\n" \
    "v_exp_f32 v114, v114\n" \
    "v_exp_f32 v115, v115\n" \
    "v_exp_f32 v116, v116\n" \
    "s_waitcnt lgkmcnt(0)\n" \
    "v_mfma_f32_32x32x16_bf16 v[96:111], v[136:139], v[128:131], 0\n" \
    "ds_write_b128 %[vw], %[vst]\n" \
    "v_exp_f32 v117, v117\n" \
    "v_exp_f32 v118, v118\n" \
    "v_exp_f32 v119, v119\n" \
    "v_exp_f32 v120, v120\n" \
    "v_exp_f32 v121, v121\n" \
    "v_smfmac_f32_32x32x32_bf16 v[80:95], v[152:155], v[112:119], %[idx]\n" \
    "v_exp_f32 v122, v122\n" \
    "v_exp_f32 v123, v123\n" \
    "v_exp_f32 v124, v124\n" \
    "v_exp_f32 v125, v125\n" \
    "v_exp_f32 v126, v126\n" \
    "v_exp_f32 v127, v127\n" \
    "s_setprio 1\n" \
    "v_exp_f32 v96, v96\n" \
    "v_exp_f32 v97, v97\n" \
    "v_smfmac_f32_32x32x32_bf16 v[80:95], v[156:159], v[120:127], %[idx]\n" \
    "ds_read_b128 v[140:143], %[ka] offset:3072\n" \
    "ds_read_b128 v[152:155], %[va] offset:3264\n" \
    "ds_read_b128 v[156:159], %[va] offset:3808\n" \
    "v_exp_f32 v98, v98\n" \
    "v_exp_f32 v99, v99\n" \
    "v_exp_f32 v100, v100\n" \
    "v_mfma_f32_32x32x16_bf16 v[112:127], v[136:139], v[132:135], 0\n" \
    "v_exp_f32 v101, v101\n" \
    "v_exp_f32 v102, v102\n" \
    "v_exp_f32 v103, v103\n" \
    "v_exp_f32 v104, v104\n" \
    "v_exp_f32 v105, v105\n" \
    "v_smfmac_f32_32x32x32_bf16 v[64:79], v[144:147], v[96:103], %[idx]\n" \
    "v_exp_f32 v106, v106\n" \
    "v_exp_f32 v107, v107\n" \
    "v_exp_f32 v108, v108\n" \
    "v_exp_f32 v109, v109\n" \
    "v_exp_f32 v110, v110\n" \
    "v_exp_f32 v111, v111\n" \
    "v_exp_f32 v112, v112\n" \
    "v_exp_f32 v113, v113\n" \
    "v_smfmac_f32_32x32x32_bf16 v[64:79], v[148:151], v[104:111], %[idx]\n" \
    "v_exp_f32 v114, v114\n" \
    "v_exp_f32 v115, v115\n" \
    "v_exp_f32 v116, v116\n" \
    "s_waitcnt lgkmcnt(0)\n" \
    "v_mfma_f32_32x32x16_bf16 v[96:111], v[140:143], v[128:131], 0\n" \
    "v_exp_f32 v117, v117\n" \
    "v_exp_f32 v118, v118\n" \
    "v_exp_f32 v119, v119\n" \
    "v_exp_f32 v120, v120\n" \
    "v_exp_f32 v121, v121\n" \
    "v_smfmac_f32_32x32x32_bf16 v[80:95], v[144:147], v[112:119], %[idx]\n" \
    "v_exp_f32 v122, v122\n" \
    "v_exp_f32 v123, v123\n" \
    "v_exp_f32 v124, v124\n" \
    "v_exp_f32 v125, v125\n" \
    "v_exp_f32 v126, v126\n" \
    "v_exp_f32 v127, v127\n" \
    "s_setprio 0\n" \
    "v_exp_f32 v96, v96\n" \
    "v_exp_f32 v97, v97\n" \
    "v_smfmac_f32_32x32x32_bf16 v[80:95], v[148:151], v[120:127], %[idx]\n" \
    "ds_read_b128 v[136:139], %[kan]\n" \
    "ds_read_b128 v[144:147], %[van]\n" \
    "ds_read_b128 v[148:151], %[van] offset:544\n" \
    "v_exp_f32 v98, v98\n" \
    "v_exp_f32 v99, v99\n" \
    "v_exp_f32 v100, v100\n" \
    "v_mfma_f32_32x32x16_bf16 v[112:127], v[140:143], v[132:135], 0\n" \
    "v_exp_f32 v101, v101\n" \
    "v_exp_f32 v102, v102\n" \
    "v_exp_f32 v103, v103\n" \
    "v_exp_f32 v104, v104\n" \
    "v_exp_f32 v105, v105\n" \
    "v_smfmac_f32_32x32x32_bf16 v[64:79], v[152:155], v[96:103], %[idx]\n" \
    "v_exp_f32 v106, v106\n" \
    "v_exp_f32 v107, v107\n" \
    "v_exp_f32 v108, v108\n" \
    "v_exp_f32 v109, v109\n" \
    "v_exp_f32 v110, v110\n" \
    "v_exp_f32 v111, v111\n" \
    "v_exp_f32 v112, v112\n" \
    "v_exp_f32 v113, v113\n" \
    "v_smfmac_f32_32x32x32_bf16 v[64:79], v[156:159], v[104:111], %[idx]\n" \
    "v_exp_f32 v114, v114\n" \
    "v_exp_f32 v115, v115\n" \
    "v_exp_f32 v116, v116\n" \
    "s_waitcnt lgkmcnt(0)\n" \
    "v_mfma_f32_32x32x16_bf16 v[96:111], v[136:139], v[128:131], 0\n" \
    "v_exp_f32 v117, v117\n" \
    "v_exp_f32 v118, v118\n" \
    "v_exp_f32 v119, v119\n" \
    "v_exp_f32 v120, v120\n" \
    "v_exp_f32 v121, v121\n" \
    "v_smfmac_f32_32x32x32_bf16 v[80:95], v[152:155], v[112:119], %[idx]\n" \
    "v_exp_f32 v122, v122\n" \
    "v_exp_f32 v123, v123\n" \
    "v_exp_f32 v124, v124\n" \
    "v_exp_f32 v125, v125\n" \
    "v_exp_f32 v126, v126\n" \
    "v_exp_f32 v127, v127\n" \
    "s_barrier\n"
#define ATT_STAGE_N \
    "s_setprio 3\n" \
    "v_exp_f32 v96, v96\n" \
    "v_exp_f32 v97, v97\n" \
    "v_smfmac_f32_32x32x32_bf16 v[80:95], v[156:159], v[120:127], %[idx]\n" \
    "ds_read_b128 v[140:143], %[ka] offset:1024\n" \
    "ds_read_b128 v[152:155], %[va] offset:1088\n" \
    "ds_read_b128 v[156:159], %[va] offset:1632\n" \
    "v_exp_f32 v98, v98\n" \
    "v_exp_f32 v99, v99\n" \
    "v_exp_f32 v100, v100\n" \
    "v_mfma_f32_32x32x16_bf16 v[112:127], v[136:139], v[132:135], 0\n" \
    "v_exp_f32 v101, v101\n" \
    "v_exp_f32 v102, v102\n" \
    "v_exp_f32 v103, v103\n" \
    "v_exp_f32 v104, v104\n" \
    "v_exp_f32 v105, v105\n" \
    "v_smfmac_f32_32x32x32_bf16 v[64:79], v[144:147], v[96:103], %[idx]\n" \
    "v_exp_f32 v106, v106\n" \
    "v_exp_f32 v107, v107\n" \
    "v_exp_f32 v108, v108\n" \
    "v_exp_f32 v109, v109\n" \
    "v_exp_f32 v110, v110\n" \
    "v_exp_f32 v111, v111\n" \
    "v_exp_f32 v112, v112\n" \
    "v_exp_f32 v113, v113\n" \
    "v_smfmac_f32_32x32x32_bf16 v[64:79], v[148:151], v[104:111], %[idx]\n" \
    "v_exp_f32 v114, v114\n" \
    "v_exp_f32 v115, v115\n" \
    "v_exp_f32 v116, v116\n" \
    "s_waitcnt lgkmcnt(0)\n" \
    "v_mfma_f32_32x32x16_bf16 v[96:111], v[140:143], v[128:131], 0\n" \
    "v_exp_f32 v117, v117\n" \
    "v_exp_f32 v118, v118\n" \
    "v_exp_f32 v119, v119\n" \
    "v_exp_f32 v120, v120\n" \
    "v_exp_f32 v121, v121\n" \
    "v_smfmac_f32_32x32x32_bf16 v[80:95], v[144:147], v[112:119], %[idx]\n" \
    "v_exp_f32 v122, v122\n" \
    "v_exp_f32 v123, v123\n" \
    "v_exp_f32 v124, v124\n" \
    "v_exp_f32 v125, v125\n" \
    "v_exp_f32 v126, v126\n" \
    "v_exp_f32 v127, v127\n" \
    "s_setprio 2\n" \
    "v_exp_f32 v96, v96\n" \
    "v_exp_f32 v97, v97\n" \
    "v_smfmac_f32_32x32x32_bf16 v[80:95], v[148:151], v[120:127], %[idx]\n" \
    "ds_read_b128 v[136:139], %[ka] offset:2048\n" \
    "ds_read_b128 v[144:147], %[va] offset:2176\n" \
    "ds_read_b128 v[148:151], %[va] offset:2720\n" \
    "v_exp_f32 v98, v98\n" \
    "v_exp_f32 v99, v99\n" \
    "v_exp_f32 v100, v100\n" \
    "v_mfma_f32_32x32x16_bf16 v[112:127], v[140:143], v[132:135], 0\n" \
    "v_exp_f32 v101, v101\n" \
    "v_exp_f32 v102, v102\n" \
    "v_exp_f32 v103, v103\n" \
    "v_exp_f32 v104, v104\n" \
    "v_exp_f32 v105, v105\n" \
    "v_smfmac_f32_32x32x32_bf16 v[64:79], v[152:155], v[96:103], %[idx]\n" \
    "v_exp_f32 v106, v106\n" \
    "v_exp_f32 v107, v107\n" \
    "v_exp_f32 v108, v108\n" \
    "v_exp_f32 v109, v109\n" \
    "v_exp_f32 v110, v110\n" \
    "v_exp_f32 v111, v111\n" \
    "v_exp_f32 v112, v112\n" \
    "v_exp_f32 v113, v113\n" \
    "v_smfmac_f32_32x32x32_bf16 v[64:79], v[156:159], v[104:111], %[idx]\n" \
    "v_exp_f32 v114, v114\n" \
    "v_exp_f32 v115, v115\n" \
    "v_exp_f32 v116, v116\n" \
    "s_waitcnt lgkmcnt(0)\n" \
    "v_mfma_f32_32x32x16_bf16 v[96:111], v[136:139], v[128:131], 0\n" \
    "v_exp_f32 v117, v117\n" \
    "v_exp_f32 v118, v118\n" \
    "v_exp_f32 v119, v119\n" \
    "v_exp_f32 v120, v120\n" \
    "v_exp_f32 v121, v121\n" \
    "v_smfmac_f32_32x32x32_bf16 v[80:95], v[152:155], v[112:119], %[idx]\n" \
    "v_exp_f32 v122, v122\n" \
    "v_exp_f32 v123, v123\n" \
    "v_exp_f32 v124, v124\n" \
    "v_exp_f32 v125, v125\n" \
    "v_exp_f32 v126, v126\n" \
    "v_exp_f32 v127, v127\n" \
    "s_setprio 1\n" \
    "v_exp_f32 v96, v96\n" \
    "v_exp_f32 v97, v97\n" \
    "v_smfmac_f32_32x32x32_bf16 v[80:95], v[156:159], v[120:127], %[idx]\n" \
    "ds_read_b128 v[140:143], %[ka] offset:3072\n" \
    "ds_read_b128 v[152:155], %[va] offset:3264\n" \
    "ds_read_b128 v[156:159], %[va] offset:3808\n" \
    "v_exp_f32 v98, v98\n" \
    "v_exp_f32 v99, v99\n" \
    "v_exp_f32 v100, v100\n" \
    "v_mfma_f32_32x32x16_bf16 v[112:127], v[136:139], v[132:135], 0\n" \
    "v_exp_f32 v101, v101\n" \
    "v_exp_f32 v102, v102\n" \
    "v_exp_f32 v103, v103\n" \
    "v_exp_f32 v104, v104\n" \
    "v_exp_f32 v105, v105\n" \
    "v_smfmac_f32_32x32x32_bf16 v[64:79], v[144:147], v[96:103], %[idx]\n" \
    "v_exp_f32 v106, v106\n" \
    "v_exp_f32 v107, v107\n" \
    "v_exp_f32 v108, v108\n" \
    "v_exp_f32 v109, v109\n" \
    "v_exp_f32 v110, v110\n" \
    "v_exp_f32 v111, v111\n" \
    "v_exp_f32 v112, v112\n" \
    "v_exp_f32 v113, v113\n" \
    "v_smfmac_f32_32x32x32_bf16 v[64:79], v[148:151], v[104:111], %[idx]\n" \
    "v_exp_f32 v114, v114\n" \
    "v_exp_f32 v115, v115\n" \
    "v_exp_f32 v116, v116\n" \
    "s_waitcnt lgkmcnt(0)\n" \
    "v_mfma_f32_32x32x16_bf16 v[96:111], v[140:143], v[128:131], 0\n" \
    "v_exp_f32 v117, v117\n" \
    "v_exp_f32 v118, v118\n" \
    "v_exp_f32 v119, v119\n" \
    "v_exp_f32 v120, v120\n" \
    "v_exp_f32 v121, v121\n" \
    "v_smfmac_f32_32x32x32_bf16 v[80:95], v[144:147], v[112:119], %[idx]\n" \
    "v_exp_f32 v122, v122\n" \
    "v_exp_f32 v123, v123\n" \
    "v_exp_f32 v124, v124\n" \
    "v_exp_f32 v125, v125\n" \
    "v_exp_f32 v126, v126\n" \
    "v_exp_f32 v127, v127\n" \
    "s_setprio 0\n" \
    "v_exp_f32 v96, v96\n" \
    "v_exp_f32 v97, v97\n" \
    "v_smfmac_f32_32x32x32_bf16 v[80:95], v[148:151], v[120:127], %[idx]\n" \
    "ds_read_b128 v[136:139], %[kan]\n" \
    "ds_read_b128 v[144:147], %[van]\n" \
    "ds_read_b128 v[148:151], %[van] offset:544\n" \
    "v_exp_f32 v98, v98\n" \
    "v_exp_f32 v99, v99\n" \
    "v_exp_f32 v100, v100\n" \
    "v_mfma_f32_32x32x16_bf16 v[112:127], v[140:143], v[132:135], 0\n" \
    "v_exp_f32 v101, v101\n" \
    "v_exp_f32 v102, v102\n" \
    "v_exp_f32 v103, v103\n" \
    "v_exp_f32 v104, v104\n" \
    "v_exp_f32 v105, v105\n" \
    "v_smfmac_f32_32x32x32_bf16 v[64:79], v[152:155], v[96:103], %[idx]\n" \
    "v_exp_f32 v106, v106\n" \
    "v_exp_f32 v107, v107\n" \
    "v_exp_f32 v108, v108\n" \
    "v_exp_f32 v109, v109\n" \
    "v_exp_f32 v110, v110\n" \
    "v_exp_f32 v111, v111\n" \
    "v_exp_f32 v112, v112\n" \
    "v_exp_f32 v113, v113\n" \
    "v_smfmac_f32_32x32x32_bf16 v[64:79], v[156:159], v[104:111], %[idx]\n" \
    "v_exp_f32 v114, v114\n" \
    "v_exp_f32 v115, v115\n" \
    "v_exp_f32 v116, v116\n" \
    "s_waitcnt lgkmcnt(0)\n" \
    "v_mfma_f32_32x32x16_bf16 v[96:111], v[136:139], v[128:131], 0\n" \
    "v_exp_f32 v117, v117\n" \
    "v_exp_f32 v118, v118\n" \
    "v_exp_f32 v119, v119\n" \
    "v_exp_f32 v120, v120\n" \
    "v_exp_f32 v121, v121\n" \
    "v_smfmac_f32_32x32x32_bf16 v[80:95], v[152:155], v[112:119], %[idx]\n" \
    "v_exp_f32 v122, v122\n" \
    "v_exp_f32 v123, v123\n" \
    "v_exp_f32 v124, v124\n" \
    "v_exp_f32 v125, v125\n" \
    "v_exp_f32 v126, v126\n" \
    "v_exp_f32 v127, v127\n" \
    "s_barrier\n"
#define ATT_STAGE_L \
    "s_setprio 3\n" \
    "v_exp_f32 v96, v96\n" \
    "v_exp_f32 v97, v97\n" \
    "v_smfmac_f32_32x32x32_bf16 v[80:95], v[156:159], v[120:127], %[idx]\n" \
    "ds_read_b128 v[140:143], %[ka] offset:1024\n" \
    "ds_read_b128 v[152:155], %[va] offset:1088\n" \
    "ds_read_b128 v[156:159], %[va] offset:1632\n" \
    "v_exp_f32 v98, v98\n" \
    "v_exp_f32 v99, v99\n" \
    "v_exp_f32 v100, v100\n" \
    "v_mfma_f32_32x32x16_bf16 v[112:127], v[136:139], v[132:135], 0\n" \
    "v_exp_f32 v101, v101\n" \
    "v_exp_f32 v102, v102\n" \
    "v_exp_f32 v103, v103\n" \
    "v_exp_f32 v104, v104\n" \
    "v_exp_f32 v105, v105\n" \
    "v_smfmac_f32_32x32x32_bf16 v[64:79], v[144:147], v[96:103], %[idx]\n" \
    "v_exp_f32 v106, v106\n" \
    "v_exp_f32 v107, v107\n" \
    "v_exp_f32 v108, v108\n" \
    "v_exp_f32 v109, v109\n" \
    "v_exp_f32 v110, v110\n" \
    "v_exp_f32 v111, v111\n" \
    "v_exp_f32 v112, v112\n" \
    "v_exp_f32 v113, v113\n" \
    "v_smfmac_f32_32x32x32_bf16 v[64:79], v[148:151], v[104:111], %[idx]\n" \
    "v_exp_f32 v114, v114\n" \
    "v_exp_f32 v115, v115\n" \
    "v_exp_f32 v116, v116\n" \
    "s_waitcnt lgkmcnt(0)\n" \
    "v_mfma_f32_32x32x16_bf16 v[96:111], v[140:143], v[128:131], 0\n" \
    "v_exp_f32 v117, v117\n" \
    "v_exp_f32 v118, v118\n" \
    "v_exp_f32 v119, v119\n" \
    "v_exp_f32 v120, v120\n" \
    "v_exp_f32 v121, v121\n" \
    "v_smfmac_f32_32x32x32_bf16 v[80:95], v[144:147], v[112:119], %[idx]\n" \
    "v_exp_f32 v122, v122\n" \
    "v_exp_f32 v123, v123\n" \
    "v_exp_f32 v124, v124\n" \
    "v_exp_f32 v125, v125\n" \
    "v_exp_f32 v126, v126\n" \
    "v_exp_f32 v127, v127\n" \
    "s_setprio 2\n" \
    "v_exp_f32 v96, v96\n" \
    "v_exp_f32 v97, v97\n" \
    "v_smfmac_f32_32x32x32_bf16 v[80:95], v[148:151], v[120:127], %[idx]\n" \
    "ds_read_b128 v[136:139], %[ka] offset:2048\n" \
    "ds_read_b128 v[144:147], %[va] offset:2176\n" \
    "ds_read_b128 v[148:151], %[va] offset:2720\n" \
    "v_exp_f32 v98, v98\n" \
    "v_exp_f32 v99, v99\n" \
    "v_exp_f32 v100, v100\n" \
    "v_mfma_f32_32x32x16_bf16 v[112:127], v[140:143], v[132:135], 0\n" \
    "v_exp_f32 v101, v101\n" \
    "v_exp_f32 v102, v102\n" \
    "v_exp_f32 v103, v103\n" \
    "v_exp_f32 v104, v104\n" \
    "v_exp_f32 v105, v105\n" \
    "v_smfmac_f32_32x32x32_bf16 v[64:79], v[152:155], v[96:103], %[idx]\n" \
    "v_exp_f32 v106, v106\n" \
    "v_exp_f32 v107, v107\n" \
    "v_exp_f32 v108, v108\n" \
    "v_exp_f32 v109, v109\n" \
    "v_exp_f32 v110, v110\n" \
    "v_exp_f32 v111, v111\n" \
    "v_exp_f32 v112, v112\n" \
    "v_exp_f32 v113, v113\n" \
    "v_smfmac_f32_32x32x32_bf16 v[64:79], v[156:159], v[104:111], %[idx]\n" \
    "v_exp_f32 v114, v114\n" \
    "v_exp_f32 v115, v115\n" \
    "v_exp_f32 v116, v116\n" \
    "s_waitcnt lgkmcnt(0)\n" \
    "v_mfma_f32_32x32x16_bf16 v[96:111], v[136:139], v[128:131], 0\n" \
    "v_exp_f32 v117, v117\n" \
    "v_exp_f32 v118, v118\n" \
    "v_exp_f32 v119, v119\n" \
    "v_exp_f32 v120, v120\n" \
    "v_exp_f32 v121, v121\n" \
    "v_smfmac_f32_32x32x32_bf16 v[80:95], v[152:155], v[112:119], %[idx]\n" \
    "v_exp_f32 v122, v122\n" \
    "v_exp_f32 v123, v123\n" \
    "v_exp_f32 v124, v124\n" \
    "v_exp_f32 v125, v125\n" \
    "v_exp_f32 v126, v126\n" \
    "v_exp_f32 v127, v127\n" \
    "s_setprio 1\n" \
    "v_exp_f32 v96, v96\n" \
    "v_exp_f32 v97, v97\n" \
    "v_smfmac_f32_32x32x32_bf16 v[80:95], v[156:159], v[120:127], %[idx]\n" \
    "ds_read_b128 v[140:143], %[ka] offset:3072\n" \
    "ds_read_b128 v[152:155], %[va] offset:3264\n" \
    "ds_read_b128 v[156:159], %[va] offset:3808\n" \
    "v_exp_f32 v98, v98\n" \
    "v_exp_f32 v99, v99\n" \
    "v_exp_f32 v100, v100\n" \
    "v_mfma_f32_32x32x16_bf16 v[112:127], v[136:139], v[132:135], 0\n" \
    "v_exp_f32 v101, v101\n" \
    "v_exp_f32 v102, v102\n" \
    "v_exp_f32 v103, v103\n" \
    "v_exp_f32 v104, v104\n" \
    "v_exp_f32 v105, v105\n" \
    "v_smfmac_f32_32x32x32_bf16 v[64:79], v[144:147], v[96:103], %[idx]\n" \
    "v_exp_f32 v106, v106\n" \
    "v_exp_f32 v107, v107\n" \
    "v_exp_f32 v108, v108\n" \
    "v_exp_f32 v109, v109\n" \
    "v_exp_f32 v110, v110\n" \
    "v_exp_f32 v111, v111\n" \
    "v_exp_f32 v112, v112\n" \
    "v_exp_f32 v113, v113\n" \
    "v_smfmac_f32_32x32x32_bf16 v[64:79], v[148:151], v[104:111], %[idx]\n" \
    "v_exp_f32 v114, v114\n" \
    "v_exp_f32 v115, v115\n" \
    "v_exp_f32 v116, v116\n" \
    "s_waitcnt lgkmcnt(0)\n" \
    "v_mfma_f32_32x32x16_bf16 v[96:111], v[140:143], v[128:131], 0\n" \
    "v_exp_f32 v117, v117\n" \
    "v_exp_f32 v118, v118\n" \
    "v_exp_f32 v119, v119\n" \
    "v_exp_f32 v120, v120\n" \
    "v_exp_f32 v121, v121\n" \
    "v_smfmac_f32_32x32x32_bf16 v[80:95], v[144:147], v[112:119], %[idx]\n" \
    "v_exp_f32 v122, v122\n" \
    "v_exp_f32 v123, v123\n" \
    "v_exp_f32 v124, v124\n" \
    "v_exp_f32 v125, v125\n" \
    "v_exp_f32 v126, v126\n" \
    "v_exp_f32 v127, v127\n" \
    "s_setprio 0\n" \
    "v_exp_f32 v96, v96\n" \
    "v_exp_f32 v97, v97\n" \
    "v_smfmac_f32_32x32x32_bf16 v[80:95], v[148:151], v[120:127], %[idx]\n" \
    "ds_read_b128 v[136:139], %[kan]\n" \
    "ds_read_b128 v[144:147], %[van]\n" \
    "ds_read_b128 v[148:151], %[van] offset:544\n" \
    "v_exp_f32 v98, v98\n" \
    "v_exp_f32 v99, v99\n" \
    "v_exp_f32 v100, v100\n" \
    "v_mfma_f32_32x32x16_bf16 v[112:127], v[140:143], v[132:135], 0\n" \
    "v_exp_f32 v101, v101\n" \
    "v_exp_f32 v102, v102\n" \
    "v_exp_f32 v103, v103\n" \
    "v_exp_f32 v104, v104\n" \
    "v_exp_f32 v105, v105\n" \
    "v_smfmac_f32_32x32x32_bf16 v[64:79], v[152:155], v[96:103], %[idx]\n" \
    "v_exp_f32 v106, v106\n" \
    "v_exp_f32 v107, v107\n" \
    "v_exp_f32 v108, v108\n" \
    "v_exp_f32 v109, v109\n" \
    "v_exp_f32 v110, v110\n" \
    "v_exp_f32 v111, v111\n" \
    "v_exp_f32 v112, v112\n" \
    "v_exp_f32 v113, v113\n" \
    "v_smfmac_f32_32x32x32_bf16 v[64:79], v[156:159], v[104:111], %[idx]\n" \
    "v_exp_f32 v114, v114\n" \
    "v_exp_f32 v115, v115\n" \
    "v_exp_f32 v116, v116\n" \
    "s_waitcnt lgkmcnt(0)\n" \
    "v_mfma_f32_32x32x16_bf16 v[96:111], v[136:139], v[128:131], 0\n" \
    "v_exp_f32 v117, v117\n" \
    "v_exp_f32 v118, v118\n" \
    "v_exp_f32 v119, v119\n" \
    "v_exp_f32 v120, v120\n" \
    "v_exp_f32 v121, v121\n" \
    "v_smfmac_f32_32x32x32_bf16 v[80:95], v[152:155], v[112:119], %[idx]\n" \
    "v_exp_f32 v122, v122\n" \
    "v_exp_f32 v123, v123\n" \
    "v_exp_f32 v124, v124\n" \
    "v_exp_f32 v125, v125\n" \
    "v_exp_f32 v126, v126\n" \
    "v_exp_f32 v127, v127\n" \
    "s_nop 1\n" \
    "v_smfmac_f32_32x32x32_bf16 v[80:95], v[156:159], v[120:127], %[idx]\n" \
    "s_setprio 0\n" \
    "s_nop 15\n"

#define ATT_ASM(STR)                                                                                                              \
    asm volatile(STR : "+{v[64:79]}"(OA), "+{v[80:95]}"(OB), "+{v[96:111]}"(SA), "+{v[112:127]}"(SB), "+{v[136:139]}"(K0),       \
                 "+{v[140:143]}"(K1), "+{v[144:147]}"(V0a), "+{v[148:151]}"(V1a), "+{v[152:155]}"(V0b), "+{v[156:159]}"(V1b)     \
                 : "{v[128:131]}"(qa), "{v[132:135]}"(qb), [idx] "v"(idx), [ka] "v"(ka), [va] "v"(va), [kan] "v"(kan),           \
                   [van] "v"(van), [kw] "v"(kw), [vw] "v"(vw), [kst] "v"(kst), [vst] "v"(vst)                                    \
                 : "memory")
template <int V>
DI void att_stage(f32x16& OA, f32x16& OB, f32x16& SA, f32x16& SB, u32x4& K0, u32x4& K1, u32x4& V0a, u32x4& V1a, u32x4& V0b, u32x4& V1b, u32x4 qa, u32x4 qb,
                  unsigned idx, unsigned ka, unsigned va, unsigned kan, unsigned van, unsigned kw, unsigned vw, u32x4 kst, u32x4 vst) {
    if constexpr (V == 0) ATT_ASM(ATT_STAGE_F); else if constexpr (V == 1) ATT_ASM(ATT_STAGE_W); else if constexpr (V == 2) ATT_ASM(ATT_STAGE_N); else ATT_ASM(ATT_STAGE_L);
}

constexpr unsigned CODE_PF = 0;

__global__ __launch_bounds__(512, 2) void fused_kernel(const uint16_t* __restrict__ x, const uint16_t* __restrict__ wq, const uint16_t* __restrict__ wk,
                                                       const uint16_t* __restrict__ wv, const uint16_t* __restrict__ wo, const uint16_t* __restrict__ w1,
                                                       const uint16_t* __restrict__ b1, const uint16_t* __restrict__ w2, const uint16_t* __restrict__ b2,
                                                       uint16_t* __restrict__ out, char* ws) {
    extern __shared__ __attribute__((aligned(16))) char lds_raw[];
    const unsigned L = (unsigned)(size_t)(LAS char*)lds_raw;
    const int tid = threadIdx.x, lane = tid & 63, w = __builtin_amdgcn_readfirstlane(tid >> 6);
    const int b = blockIdx.x, p = (b & 7) * 4 + (b >> 6), g = (b >> 3) & 7;
    const int l31 = lane & 31, h = lane >> 5, l15 = lane & 15, q4 = lane >> 4;
    const __amdgpu_buffer_rsrc_t rs = __builtin_amdgcn_make_buffer_rsrc(ws, 0, WS_BYTES, 0x00020000);
    STAMP(0);
    const int nft = g < 3 ? 6 : 5, ft0 = g < 3 ? 6 * g : 5 * g + 3;
    const int ft = ft0 + w - 2;
    const bool act = w < 2 + nft;
    const int th3 = w & 1, kq = w >> 1;
    unsigned ep, b1raw; u32x2 b2a, b2b;
    {
        const unsigned b1o = (unsigned)((w >= 2 && act) ? ft * 32 + l31 : l31) * 2u, b2o = (unsigned)(g * 64 + kq * 16 + h * 4) * 2u;
        asm volatile("global_load_dword %0, %4, %5 sc1\n\tglobal_load_ushort %1, %6, %7\n\tglobal_load_dwordx2 %2, %8, %9\n\tglobal_load_dwordx2 %3, %8, %9 offset:16"
                     : "=&v"(ep), "=&v"(b1raw), "=&v"(b2a), "=&v"(b2b)
                     : "v"(0u), "s"(ws), "v"(b1o), "s"(b1), "v"(b2o), "s"(b2) : "memory");
    }
    {
        const char* xs = (const char*)x + (size_t)(p * 64 + w * 8) * 1024;
        static_for<8>([&](auto I) { glds(lane * 16, xs + I * 1024, L + L_X + (w * 8 + I) * XP); });
        const char* ks = (const char*)((g & 1) ? wv : wk) + w * 2048;
        glds(lane * 16, ks, L + L_WKV + w * 2048);
        glds(lane * 16, ks + 1024, L + L_WKV + w * 2048 + 1024);
    }
    const unsigned wpitch = w < 2 ? 1024u : 2752u;
    const char* wsrc = w < 2 ? (const char*)wq + (g * 64 + w * 32) * 2 : (const char*)w1 + (act ? ft : 0) * 64;
    const unsigned woff = (lane >> 2) * wpitch + (lane & 3) * 16, wring = L + L_RING + w * RING * 1024;
    static_for<RING>([&](auto I) { glds(woff, wsrc + (size_t)I * 16 * wpitch, wring + I * 1024); });
    if (tid < 2) lw32(L + L_MISC + tid * 4, 0u);
    const u32x4 ONES = {0x3F803F80u, 0x3F803F80u, 0x3F803F80u, 0x3F803F80u}, ZERO4 = {0u, 0u, 0u, 0u};
    if (lane < 4) lw128(L + 8192 + w * VT + (lane >> 1) * VS + (lane & 1) * 16, (lane & 1) ? ZERO4 : ONES);
    asm volatile("s_waitcnt vmcnt(%4) lgkmcnt(0)\n\ts_barrier" : "+v"(ep), "+v"(b1raw), "+v"(b2a), "+v"(b2b) : "n"(RING) : "memory");
    STAMP(1);
    const unsigned tag = ep + 1u;
    const int tg = g >> 1, tkv = tg >> 1;
    if (w == 0) {
        const unsigned xa = L + L_X + (16 * tg + l15) * XP + q4 * 16, wa = L + L_WKV + (q4 * 8 + (l15 >> 2)) * 32 + (lane & 3) * 8;
        f32x4 acc = {0.f, 0.f, 0.f, 0.f}; float cs = 0.f;
        if (g & 1) {
#pragma unroll
            for (int ks = 0; ks < 16; ++ks) { const u32x4 xf = lr128(xa + ks * 64), wf = trr2(wa + ks * 1024, 128); acc = mfma16(xf, wf, acc);
                cs = dot2(wf[0], ONES[0], cs); cs = dot2(wf[1], ONES[0], cs); cs = dot2(wf[2], ONES[0], cs); cs = dot2(wf[3], ONES[0], cs); }
        } else {
#pragma unroll
            for (int ks = 0; ks < 16; ++ks) { const u32x4 xf = lr128(xa + ks * 64), wf = trr2(wa + ks * 1024, 128); acc = mfma16(wf, xf, acc);
                cs = dot2(wf[0], ONES[0], cs); cs = dot2(wf[1], ONES[0], cs); cs = dot2(wf[2], ONES[0], cs); cs = dot2(wf[3], ONES[0], cs); }
        }
        cs = add32(add16(cs));
        lds_wait_cnt(L + L_MISC, 4);
        float mu, sg, rstd; ln_stats(L + L_ST + tkv * 2048 + ((tg & 1) * 16 + l15) * 64, mu, sg, rstd);
        const u32x4 we = q4 == 0 ? ext_c(cs) : ZERO4, xe = q4 == 0 ? ext_m(mu) : ZERO4;
        const unsigned tile = 2 * p + tkv; float kn = 0.f;
        if (g & 1) {
            acc = mfma16(xe, we, acc);
            const float r0 = __shfl(rstd, 4 * q4), r1 = __shfl(rstd, 4 * q4 + 1), r2 = __shfl(rstd, 4 * q4 + 2), r3 = __shfl(rstd, 4 * q4 + 3);
            const u32x2 o = {cvtpk(acc[0] * r0, acc[1] * r1), cvtpk(acc[2] * r2, acc[3] * r3)};
            ST64(o, WS_V + tile * 1024 + (tg & 1) * 512 + (q4 >> 1) * 256 + l15 * 16 + (q4 & 1) * 8);
        } else {
            acc = mfma16(we, xe, acc);
            const u32x2 o = {cvtpk(acc[0] * rstd, acc[1] * rstd), cvtpk(acc[2] * rstd, acc[3] * rstd)};
            ST64(o, WS_K + tile * 1024 + (q4 & 1) * 512 + ((tg & 1) * 16 + l15) * 16 + (q4 >> 1) * 8);
            kn = rowmax16(add32(add16(dot2(o[0], o[0], dot2(o[1], o[1], 0.f)))));
        }
        waitv<0>();
        if (lane == 0) ST64(((u32x2){tag, __float_as_uint(kn)}), WS_KVF + b * 8);
        STAMP(2);
    }
    {
        const int tl = w >= 4 ? tkv : 1 - tkv, kr = w & 3;
        const unsigned xa = L + L_X + (32 * tl + l31) * XP + h * 16 + kr * 256;
        float s1 = 0.f, s2 = 0.f, s1b = 0.f, s2b = 0.f;
#pragma unroll
        for (int i = 0; i < 8; ++i) { const u32x4 v = lr128(xa + i * 32);
            s1 = dot2(v[0], ONES[0], s1); s2 = dot2(v[0], v[0], s2); s1b = dot2(v[1], ONES[0], s1b); s2b = dot2(v[1], v[1], s2b);
            s1 = dot2(v[2], ONES[0], s1); s2 = dot2(v[2], v[2], s2); s1b = dot2(v[3], ONES[0], s1b); s2b = dot2(v[3], v[3], s2b); }
        lw64(L + L_ST + tl * 2048 + l31 * 64 + (kr * 2 + h) * 8, (u32x2){__float_as_uint(s1 + s1b), __float_as_uint(s2 + s2b)});
        if (lane == 0) asm volatile("ds_add_u32 %0, %1" ::"v"(L + L_MISC + (w >= 4 ? 0 : 4)), "v"(1u) : "memory");
    }
    f32x16 acc0 = {}, acc1 = {};
    float cs = 0.f, cs2 = 0.f, rs0, rs1, sg0, sg1; u32x4 xe0, xe1;
    u32x4 fl0, fl1;
    {
        const unsigned xa = L + L_X + l31 * XP + h * 16;
        const unsigned wa = wring + (h * 8 + (l15 >> 2)) * 64 + (q4 & 1) * 32 + (lane & 3) * 8;
        u32x4 wf[2], x0[2], x1[2];
        waitv<RING - 1>();
        wf[0] = trr2(wa, 256); x0[0] = lr128(xa); x1[0] = lr128(xa + 32 * XP);
        static_for<32>([&](auto I) {
            constexpr int k = I, c = k & 1, n = c ^ 1;
            if constexpr (k + 1 < 32) {
                constexpr int last = (k + RING - 1 < 31 ? k + RING - 1 : 31);
                waitv<(last - (k + 1)) + (k >= 27 ? 2 : 0)>();
                wf[n] = trr2(wa + ((k + 1) % RING) * 1024, 256); x0[n] = lr128(xa + (k + 1) * 32); x1[n] = lr128(xa + 32 * XP + (k + 1) * 32);
            }
            acc0 = mfma32(wf[c], x0[c], acc0); acc1 = mfma32(wf[c], x1[c], acc1);
            cs = dot2(wf[c][0], ONES[0], cs); cs2 = dot2(wf[c][1], ONES[0], cs2); cs = dot2(wf[c][2], ONES[0], cs); cs2 = dot2(wf[c][3], ONES[0], cs2);
            if constexpr (k + RING < 32) {
                USE(wf[c]);
                glds(woff, wsrc + (size_t)(k + RING) * 16 * wpitch, wring + (k % RING) * 1024);
            }
            if constexpr (k == 26) { fl0 = LD128(WS_KVF + lane * 32); fl1 = LD128(WS_KVF + lane * 32 + 16); CFENCE(); }
            if constexpr (k == 12) {
                lds_wait_cnt(L + L_MISC, 4); lds_wait_cnt(L + L_MISC + 4, 4);
                float m0, m1; ln_stats(L + L_ST + l31 * 64, m0, sg0, rs0); ln_stats(L + L_ST + 2048 + l31 * 64, m1, sg1, rs1);
                xe0 = h ? ext_s(sg0) : ext_m(m0); xe1 = h ? ext_s(sg1) : ext_m(m1);
            }
        });
    }
    STAMP(3);
    {
        const float c = add32(cs + cs2);
        const unsigned bb = (w >= 2) ? (b1raw | (b1raw << 16)) : 0u;
        const u32x4 we = h ? (u32x4){bb, bb & 0xFFFFu, 0u, 0u} : ext_c(c);
        acc0 = mfma32(we, xe0, acc0); acc1 = mfma32(we, xe1, acc1);
    }
    float kmax2;
    for (unsigned it = 0;; ++it) {
        const bool ok = fl0[0] == tag && fl0[2] == tag && fl1[0] == tag && fl1[2] == tag;
        if (__all(ok) || it >= SPIN_CAP) break;
        __builtin_amdgcn_s_sleep(2);
        fl0 = LD128(WS_KVF + lane * 32); fl1 = LD128(WS_KVF + lane * 32 + 16);
    }
    CFENCE();
    STAMP(4);
    const unsigned kvg = (unsigned)((w >> 2) * 32 + (w & 3)) * 1024u + lane * 16;
    u32x4 kc0 = LD128(WS_K + kvg), vc0 = LD128(WS_V + kvg), kc1 = LD128(WS_K + kvg + 4096), vc1 = LD128(WS_V + kvg + 4096);
    u32x4 kA = LD128(WS_K + kvg + 8192), vA = LD128(WS_V + kvg + 8192), kB, vB;
    kmax2 = wavemax(fmaxf(fmaxf(__uint_as_float(fl0[1]), __uint_as_float(fl0[3])), fmaxf(__uint_as_float(fl1[1]), __uint_as_float(fl1[3]))));
    if (b == 0 && tid == 0) ST32(tag, WS_EPOCH);
    u32x2 xr0, xr1;
    { const unsigned a = L + L_X + (32 * th3 + l31) * XP + (g * 64 + kq * 16 + h * 4) * 2; xr0 = lr64(a); xr1 = lr64(a + 16); }
    if (w < 2) {
        const float c0 = rs0 * QSCALE, c1 = rs1 * QSCALE; float qn = 0.f;
        static_for<4>([&](auto I) {
            constexpr int t = I >> 1, hd = I & 1; const f32x16& a = t ? acc1 : acc0; const float c = t ? c1 : c0;
            const u32x4 f = {cvtpk(a[8 * hd] * c, a[8 * hd + 1] * c), cvtpk(a[8 * hd + 2] * c, a[8 * hd + 3] * c), cvtpk(a[8 * hd + 4] * c, a[8 * hd + 5] * c), cvtpk(a[8 * hd + 6] * c, a[8 * hd + 7] * c)};
            lw128(L + L_Q + ((w * 2 + t) * 2 + hd) * 1024 + lane * 16, f);
            qn = fmaxf(qn, add32(dot2(f[0], f[0], dot2(f[1], f[1], dot2(f[2], f[2], dot2(f[3], f[3], 0.f))))));
        });
        qn = max16(rowmax16(qn));
        if (lane == 0) lw32(L + L_MISC + 8 + w * 4, __float_as_uint(qn));
    } else if (act) {
        static_for<2>([&](auto I) {
            constexpr int t = I; const f32x16& a = t ? acc1 : acc0; const float r = t ? rs1 : rs0, sg = t ? sg1 : sg0;
            const float C0 = -2.3022081986f * r, C1 = -0.1029432397f * r * r * r;
            float y[16];
#pragma unroll
            for (int i = 0; i < 16; ++i) { const float v = a[i], z = v * __builtin_fmaf(v * v, C1, C0), e = __builtin_amdgcn_exp2f(z); y[i] = v * __builtin_amdgcn_rcpf(__builtin_fmaf(e, sg, sg)); }
#pragma unroll
            for (int s = 0; s < 2; ++s) {
                const u32x4 f = {cvtpk(y[8 * s], y[8 * s + 1]), cvtpk(y[8 * s + 2], y[8 * s + 3]), cvtpk(y[8 * s + 4], y[8 * s + 5]), cvtpk(y[8 * s + 6], y[8 * s + 7])};
                ST128(f, WS_H + (unsigned)((p * 2 + t) * 86 + 2 * ft + s) * 1024u + lane * 16);
            }
        });
        waitv<0>();
        if (lane == 0) ST32(tag, WS_HF + p * 256 + ft * 4);
    }
    const unsigned kwo = L + w * 1024 + lane * 16, vwo = L + 8192 + w * VT + (VS - 512) + h * VS + l31 * 16;
    lw128(kwo, kc0); lw128(vwo, vc0);
    PIN(kc1); PIN(vc1); PIN(kA); PIN(vA); PIN(xr0); PIN(xr1);
    STAMP(5);
    LBAR();
    STAMP(6);
    lw128(kwo + SLOT, kc1); lw128(vwo + SLOT, vc1);
    if (lane < 8) lw128(L + (1 + (lane >> 2)) * SLOT + 8192 + w * VT + ((lane >> 1) & 1) * VS + (lane & 1) * 16, (lane & 1) ? ZERO4 : ONES);
    const int tq = w & 3, kh = w >> 2;
    const u32x4 qa = lr128(L + L_Q + (((tq >> 1) * 2 + (tq & 1)) * 2) * 1024 + lane * 16), qb = lr128(L + L_Q + (((tq >> 1) * 2 + (tq & 1)) * 2 + 1) * 1024 + lane * 16);
    const u32x2 qn2 = lr64(L + L_MISC + 8);
    const float bound2 = fmaxf(__uint_as_float(qn2[0]), __uint_as_float(qn2[1])) * kmax2;
    const bool fast = __builtin_amdgcn_readfirstlane(bound2 <= SAFE_LOG2 * SAFE_LOG2 ? 1 : 0) != 0;
    unsigned idx = 0xDDDDDDDDu; PIN(idx);
    f32x16 OA = {}, OB = {};
    float mA = 0.f, mB = 0.f;
    const int ks0 = kq < 2 ? 22 * kq : 21 * kq + 2, kcnt = kq < 2 ? 22 : 21;
    const unsigned w3off = (unsigned)((((lane >> 4) & 1) * 8 + 4 * th3 + ((lane >> 2) & 3)) * 1024 + g * 128 + (lane >> 5) * 64 + (lane & 3) * 16);
    auto w3dma = [&](auto J) {
        static_for<3>([&](auto S) {
            constexpr int i = 3 * J + S;
            const char* src = i < 22 ? (const char*)w2 + (size_t)(ks0 + (i < 21 ? i : kcnt - 1)) * 16384 : (const char*)wo + (size_t)(8 * kq + i - 22) * 16384;
            glds(w3off, src, L + L_W + ((J + 1) % 5) * WSL + kq * 6144 + S * 2048 + th3 * 1024);
        });
    };
    u32x4 hf[22]; unsigned hfl;
    const unsigned hgo = WS_H + (unsigned)((p * 2 + th3) * 86 + ks0) * 1024u + lane * 16;
    auto hwait = [&]() {
        for (unsigned it = 0;; ++it) {
            if (__all(lane >= 43 || hfl == tag) || it >= SPIN_CAP) break;
            __builtin_amdgcn_s_sleep(2);
            hfl = LD32(WS_HF + p * 256 + lane * 4);
        }
        CFENCE();
    };
    if (fast) {
        const unsigned kro = L + kh * 4096 + lane * 16, vro = L + 8192 + kh * 4 * VT + (l31 < 16 ? (VS - 512) + h * 256 + l31 * 16 : ((l31 & 27) == 16 ? 0 : 16));
        u32x4 K0 = lr128(kro), V0a = lr128(vro), V1a = lr128(vro + VS), K1 = ZERO4, V0b = ZERO4, V1b = ZERO4;
        f32x16 SA, SB = {};
        asm volatile("v_mfma_f32_32x32x16_bf16 v[96:111], v[136:139], v[128:131], 0\n\ts_nop 15" : "={v[96:111]}"(SA) : "{v[136:139]}"(K0), "{v[128:131]}"(qa));
        static_for<8>([&](auto C) {
            constexpr int c = C;
            u32x4& kst = (c & 1) ? kB : kA; u32x4& vst = (c & 1) ? vB : vA;
            if constexpr (c < 6) { PIN(kst); PIN(vst); }
            if constexpr (c == 2) { PIN(hfl); hwait(); }
            if constexpr (c >= 3 && c <= 6) static_for<6>([&](auto I) { constexpr int i = (c - 3) * 6 + I; if constexpr (i < 22) PIN(hf[i]); });
            if constexpr (c < 5) { u32x4& kn = (c & 1) ? kA : kB; u32x4& vn = (c & 1) ? vA : vB; kn = LD128(WS_K + kvg + (c + 3) * 4096); vn = LD128(WS_V + kvg + (c + 3) * 4096); }
            if constexpr (c == 1) hfl = LD32(WS_HF + p * 256 + lane * 4);
            if constexpr (c >= 2 && c <= 5) static_for<6>([&](auto I) { constexpr int i = (c - 2) * 6 + I; if constexpr (i < 21) hf[i] = LD128(hgo + i * 1024); if constexpr (i == 21) hf[i] = LD128(kq < 2 ? hgo + i * 1024 : WS_BYTES); });
            if constexpr (c < 4) w3dma(C);
            const unsigned ka = kro + (c % 3) * SLOT, va = vro + (c % 3) * SLOT, kan = kro + ((c + 1) % 3) * SLOT, van = vro + ((c + 1) % 3) * SLOT;
            const unsigned kw = kwo + ((c + 2) % 3) * SLOT, vw = vwo + ((c + 2) % 3) * SLOT;
            att_stage<(c == 0 ? 0 : c < 6 ? 1 : c == 6 ? 2 : 3)>(OA, OB, SA, SB, K0, K1, V0a, V1a, V0b, V1b, qa, qb, idx, ka, va, kan, van, kw, vw, c < 6 ? kst : qa, c < 6 ? vst : qa);
        });
    } else {
        static_for<4>([&](auto J) { w3dma(J); });
        mA = -INFINITY; mB = -INFINITY;
        for (int t = 0; t < 32; ++t) {
            const unsigned o = (unsigned)(kh * 32 + t) * 1024u;
            const u32x4 kf = LD128(WS_K + o + lane * 16);
            u32x4 v0 = LD128(WS_V + o + h * 256 + (l31 & 15) * 16), v1 = LD128(WS_V + o + 512 + h * 256 + (l31 & 15) * 16);
            if (l31 >= 16) { v0 = ONES; v1 = ONES; }
            static_for<2>([&](auto I) {
                f32x16& O = I ? OB : OA; float& m = I ? mB : mA;
                f32x16 s = mfma32(kf, I ? qb : qa, (f32x16){});
                float mx = s[0];
#pragma unroll
                for (int i = 1; i < 16; ++i) mx = fmaxf(mx, s[i]);
                const float mn = fmaxf(m, max32(mx)), al = __builtin_amdgcn_exp2f(m - mn); m = mn;
#pragma unroll
                for (int i = 0; i < 16; ++i) { O[i] *= al; s[i] = __builtin_amdgcn_exp2f(s[i] - mn); }
                O = smf(v0, __builtin_shufflevector(s, s, 0, 1, 2, 3, 4, 5, 6, 7), O, idx);
                O = smf(v1, __builtin_shufflevector(s, s, 8, 9, 10, 11, 12, 13, 14, 15), O, idx);
            });
        }
        hfl = LD32(WS_HF + p * 256 + lane * 4); hwait();
        static_for<22>([&](auto I) { constexpr int i = I; hf[i] = LD128((i < 21 || kq < 2) ? hgo + i * 1024 : WS_BYTES); });
        asm volatile("s_waitcnt vmcnt(0) lgkmcnt(0)\n\ts_barrier" ::: "memory");
    }
    STAMP(7);
    {
        const f32x16& snd = kh ? OA : OB; const f32x16& kp = kh ? OB : OA;
        const float ms = kh ? mA : mB, mk = kh ? mB : mA;
        const unsigned wa = L + w * 2048 + lane * 16, ra = L + (w ^ 4) * 2048 + lane * 16;
        lw128(wa, (u32x4){__float_as_uint(snd[0]), __float_as_uint(snd[1]), __float_as_uint(snd[2]), __float_as_uint(snd[3])});
        lw128(wa + 1024, (u32x4){__float_as_uint(snd[4]), __float_as_uint(snd[5]), __float_as_uint(snd[6]), __float_as_uint(snd[7])});
        lw64(L + L_ST + w * 512 + lane * 8, (u32x2){__float_as_uint(snd[8]), __float_as_uint(ms)});
        asm volatile("s_waitcnt vmcnt(0) lgkmcnt(0)\n\ts_barrier" ::: "memory");
        const u32x4 r0 = lr128(ra), r1 = lr128(ra + 1024); const u32x2 r2 = lr64(L + L_ST + (w ^ 4) * 512 + lane * 8);
        float f1 = 1.f, f2 = 1.f;
        if (!fast) { const float mo = __uint_as_float(r2[1]), M = fmaxf(mk, mo); f1 = __builtin_amdgcn_exp2f(mk - M); f2 = __builtin_amdgcn_exp2f(mo - M); }
        const float inv = __builtin_amdgcn_rcpf(kp[8] * f1 + __uint_as_float(r2[0]) * f2);
        f1 *= inv; f2 *= inv;
        float o[8];
#pragma unroll
        for (int i = 0; i < 4; ++i) { o[i] = kp[i] * f1 + __uint_as_float(r0[i]) * f2; o[4 + i] = kp[4 + i] * f1 + __uint_as_float(r1[i]) * f2; }
        const int head = 4 * g + (tq >> 1) * 2 + kh;
        ST128(((u32x4){cvtpk(o[0], o[1]), cvtpk(o[2], o[3]), cvtpk(o[4], o[5]), cvtpk(o[6], o[7])}), WS_O + (unsigned)((p * 2 + (tq & 1)) * 32 + head) * 1024u + lane * 16);
        waitv<0>();
        if (lane == 0) ST32(tag, WS_OF + p * 256 + ((tq & 1) * 32 + head) * 4);
    }
    STAMP(8);
    w3dma(std::integral_constant<int, 4>{});
    f32x16 c0 = {}, c1 = {};
    {
        float xi[8];
#pragma unroll
        for (int i = 0; i < 2; ++i) {
            xi[2 * i] = __uint_as_float(xr0[i] << 16) + __uint_as_float(b2a[i] << 16); xi[2 * i + 1] = __uint_as_float(xr0[i] & 0xFFFF0000u) + __uint_as_float(b2a[i] & 0xFFFF0000u);
            xi[4 + 2 * i] = __uint_as_float(xr1[i] << 16) + __uint_as_float(b2b[i] << 16); xi[5 + 2 * i] = __uint_as_float(xr1[i] & 0xFFFF0000u) + __uint_as_float(b2b[i] & 0xFFFF0000u);
        }
#pragma unroll
        for (int i = 0; i < 8; ++i) { c0[i] = kq == 0 ? xi[i] : 0.f; c0[8 + i] = kq == 1 ? xi[i] : 0.f; c1[i] = kq == 2 ? xi[i] : 0.f; c1[8 + i] = kq == 3 ? xi[i] : 0.f; }
    }
    u32x4 of[8]; unsigned ofl;
    const unsigned wa3 = L + L_W + kq * 6144 + h * 1024 + (l15 >> 2) * 64 + (q4 & 1) * 32 + (lane & 3) * 8;
    static_for<10>([&](auto J) {
        constexpr int j = J;
        static_for<3>([&](auto S) {
            constexpr int i = 3 * j + S;
            const unsigned a = wa3 + ((j + 1) % 5) * WSL + S * 2048;
            const u32x4 a0 = trr2(a, 256), a1 = trr2(a + 512, 256);
            const u32x4 bf = i < 22 ? hf[i < 22 ? i : 0] : of[i < 22 ? 0 : i - 22];
            c0 = mfma32(a0, bf, c0); c1 = mfma32(a1, bf, c1);
        });
        if constexpr (j == 4) {
            ofl = LD32(WS_OF + p * 256 + (th3 * 32 + 8 * kq + (lane & 7)) * 4);
            for (unsigned it = 0;; ++it) {
                if (__all(ofl == tag) || it >= SPIN_CAP) break;
                __builtin_amdgcn_s_sleep(1);
                ofl = LD32(WS_OF + p * 256 + (th3 * 32 + 8 * kq + (lane & 7)) * 4);
            }
            CFENCE();
            STAMP(9);
            static_for<8>([&](auto I) { of[I] = LD128(WS_O + (unsigned)((p * 2 + th3) * 32 + 8 * kq + I) * 1024u + lane * 16); });
        }
        if constexpr (j < 9) {
            PIN(c0); PIN(c1);
            if constexpr (j == 3) waitv<9>(); else if constexpr (j == 8) waitv<0>();
            LBAR();
            if constexpr (j < 5) w3dma(std::integral_constant<int, j + 5>{});
        }
    });
    STAMP(10);
    LBAR();
    {
        const unsigned base = L + th3 * 32768 + lane * 16;
        static_for<4>([&](auto Q) {
            constexpr int q = Q; const f32x16& c = q < 2 ? c0 : c1; constexpr int o = (q & 1) * 8;
            if (q != kq) {
                lw128(base + (q * 4 + kq) * 2048, (u32x4){__float_as_uint(c[o]), __float_as_uint(c[o + 1]), __float_as_uint(c[o + 2]), __float_as_uint(c[o + 3])});
                lw128(base + (q * 4 + kq) * 2048 + 1024, (u32x4){__float_as_uint(c[o + 4]), __float_as_uint(c[o + 5]), __float_as_uint(c[o + 6]), __float_as_uint(c[o + 7])});
            }
        });
        float y[8];
#pragma unroll
        for (int i = 0; i < 8; ++i) y[i] = kq == 0 ? c0[i] : kq == 1 ? c0[8 + i] : kq == 2 ? c1[i] : c1[8 + i];
        LBAR();
#pragma unroll
        for (int s = 1; s < 4; ++s) {
            const unsigned a = base + (kq * 4 + ((kq + s) & 3)) * 2048;
            const u32x4 r0 = lr128(a), r1 = lr128(a + 1024);
#pragma unroll
            for (int i = 0; i < 4; ++i) { y[i] += __uint_as_float(r0[i]); y[4 + i] += __uint_as_float(r1[i]); }
        }
        unsigned a0 = cvtpk(y[0], y[1]), a1 = cvtpk(y[2], y[3]), e0 = cvtpk(y[4], y[5]), e1 = cvtpk(y[6], y[7]);
        auto s0 = __builtin_amdgcn_permlane32_swap(a0, e0, false, false); auto s1 = __builtin_amdgcn_permlane32_swap(a1, e1, false, false);
        *(u32x4*)(out + (size_t)(p * 64 + th3 * 32 + l31) * 512 + g * 64 + kq * 16 + h * 8) = (u32x4){s0[0], s1[0], s0[1], s1[1]};
    }
    STAMP(11);
}

extern "C" void kernel_launch(void* const* d_in, const int* in_sizes, int n_in, void* d_out, int out_size, void* d_ws, size_t ws_size, hipStream_t stream) {
    (void)in_sizes; (void)n_in; (void)out_size; (void)ws_size;
    (void)hipFuncSetAttribute((const void*)fused_kernel, hipFuncAttributeMaxDynamicSharedMemorySize, LDS_BYTES);
    fused_kernel<<<256, 512, LDS_BYTES, stream>>>((const uint16_t*)d_in[0], (const uint16_t*)d_in[1], (const uint16_t*)d_in[2], (const uint16_t*)d_in[3],
                                                  (const uint16_t*)d_in[4], (const uint16_t*)d_in[6], (const uint16_t*)d_in[7], (const uint16_t*)d_in[8],
                                                  (const uint16_t*)d_in[9], (uint16_t*)d_out, (char*)d_ws);
}
```

```cpp
#include <hip/hip_runtime.h>
#include <hip/hip_bf16.h>
#include <stdint.h>
#include <utility>

typedef __attribute__((ext_vector_type(16))) float f32x16;
typedef __attribute__((ext_vector_type(8))) float f32x8;
typedef __attribute__((ext_vector_type(4))) float f32x4;
typedef __attribute__((ext_vector_type(8))) __bf16 bf16x8;
typedef __attribute__((ext_vector_type(16))) __bf16 bf16x16;
typedef __attribute__((ext_vector_type(2))) __bf16 bf16x2;
typedef __attribute__((ext_vector_type(4))) unsigned u32x4;
typedef __attribute__((ext_vector_type(2))) unsigned u32x2;
#define LAS __attribute__((address_space(3)))
#define DI __device__ __forceinline__

constexpr unsigned WS_EPOCH = 0, WS_KVF = 4096, WS_HF = 8192, WS_OF = 16384, WS_K = 65536, WS_V = 131072, WS_CS = 262144, WS_H = 524288,
                   WS_O = WS_H + 32 * 2 * 86 * 1024, WS_BYTES = WS_O + 32 * 2 * 32 * 1024;
constexpr unsigned VS = 544, VT = 2 * VS, SLOT = 8192 + 8 * VT;
constexpr unsigned L_WKV = 0, L_RING = SLOT, RING = 8, L_X = L_RING + 8 * RING * 1024, XP = 1040;
constexpr unsigned WSL = 24576, L_W = 2 * SLOT;
constexpr unsigned L_Q = 3 * SLOT, L_SCR = L_X + 64 * XP, L_ST = L_SCR + 8192, L_XE = L_ST + 4096, L_MISC = L_X + 1024  , LDS_BYTES = 160 * 1024;
static_assert(WS_OF + 32768 <= WS_K && ((L_ST + 512) & 0x7FF) == 0, "flag map");
static_assert(L_W + WSL <= L_Q + 8192 && L_Q + 8192 + 4 * WSL <= L_ST && L_Q >= L_RING + 32768 && L_Q + 8192 <= L_RING + 49152 && L_XE + 2560 <= LDS_BYTES, "lds map");
constexpr unsigned SPIN_CAP = 1u << 18;
constexpr int PRE = 2;

template <class F, int... I> DI void static_for_(F&& f, std::integer_sequence<int, I...>) { (f(std::integral_constant<int, I>{}), ...); }
template <int N, class F> DI void static_for(F&& f) { static_for_(f, std::make_integer_sequence<int, N>{}); }

extern "C" __device__ unsigned long long dispatch_id() __asm("llvm.amdgcn.dispatch.id");
DI unsigned cvtpk(float lo, float hi) { bf16x2 v = {(__bf16)lo, (__bf16)hi}; return __builtin_bit_cast(unsigned, v); }
DI float bfr(float x) { return __uint_as_float(cvtpk(x, x) & 0xFFFF0000u); }
DI void lw32(unsigned a, unsigned v) { *(LAS unsigned*)(size_t)a = v; }
DI f32x16 mfma32(u32x4 a, u32x4 b, f32x16 c) { return __builtin_amdgcn_mfma_f32_32x32x16_bf16(__builtin_bit_cast(bf16x8, a), __builtin_bit_cast(bf16x8, b), c, 0, 0, 0); }
DI f32x16 smf(u32x4 a, f32x8 b, f32x16 c, unsigned idx) { return __builtin_amdgcn_smfmac_f32_32x32x32_bf16(__builtin_bit_cast(bf16x8, a), __builtin_bit_cast(bf16x16, b), c, idx, 0, 0); }
DI void glds(unsigned voff, const void* sbase, unsigned dst) {
    asm volatile("s_mov_b32 m0, %2\n\ts_nop 0\n\tglobal_load_lds_dwordx4 %0, %1" ::"v"(voff), "s"(sbase), "s"(dst) : "memory");
}
DI void touch(unsigned voff, const void* sbase, unsigned dst) {
    asm volatile("s_mov_b32 m0, %2\n\ts_nop 0\n\tglobal_load_lds_dword %0, %1" ::"v"(voff), "s"(sbase), "s"(dst) : "memory");
}
template <int N> DI void waitv() { asm volatile("s_waitcnt vmcnt(%0)" ::"n"(N) : "memory"); }
template <class T> DI void PIN(T& x) { asm volatile("" : "+v"(x)); }
DI void CFENCE() { asm volatile("" ::: "memory"); }
DI float sw32(float x, float& o) { auto s = __builtin_amdgcn_permlane32_swap(__float_as_uint(x), __float_as_uint(x), false, false); o = __uint_as_float(s[1]); return __uint_as_float(s[0]); }
DI float add32(float x) { float o, a = sw32(x, o); return a + o; }
DI float max32(float x) { float o, a = sw32(x, o); return fmaxf(a, o); }
DI u32x4 ext_c(float c) { const float a = bfr(c), r = c - a, b = bfr(r), d = r - b; return (u32x4){cvtpk(a, a), cvtpk(b, a), cvtpk(b, d), 0u}; }
#define LD128(off) __builtin_amdgcn_raw_buffer_load_b128(rs, (off), 0, 16)
#define ST128(v, off) __builtin_amdgcn_raw_buffer_store_b128((v), rs, (off), 0, 16)
#define ST32(v, off) __builtin_amdgcn_raw_buffer_store_b32((v), rs, (off), 0, 16)


#define KV_ASM \
    "s_setprio 2\n" \
    "ds_read_b64_tr_b16 v[96:97], %[wa] offset:0\n" \
    "ds_read_b64_tr_b16 v[98:99], %[wa] offset:128\n" \
    "ds_read_b128 v[120:123], %[xa] offset:0\n" \
    "ds_read_b64_tr_b16 v[100:101], %[wa] offset:1024\n" \
    "ds_read_b64_tr_b16 v[102:103], %[wa] offset:1152\n" \
    "ds_read_b128 v[124:127], %[xa] offset:64\n" \
    "ds_read_b64_tr_b16 v[104:105], %[wa] offset:2048\n" \
    "ds_read_b64_tr_b16 v[106:107], %[wa] offset:2176\n" \
    "ds_read_b128 v[128:131], %[xa] offset:128\n" \
    "ds_read_b64_tr_b16 v[108:109], %[wa] offset:3072\n" \
    "ds_read_b64_tr_b16 v[110:111], %[wa] offset:3200\n" \
    "ds_read_b128 v[132:135], %[xa] offset:192\n" \
    "ds_read_b64_tr_b16 v[112:113], %[wa] offset:4096\n" \
    "ds_read_b64_tr_b16 v[114:115], %[wa] offset:4224\n" \
    "ds_read_b128 v[136:139], %[xa] offset:256\n" \
    "v_mov_b32 v72, 0x3f803f80\n" \
    "v_mov_b32 v73, 0x3f803f80\n" \
    "v_mov_b32 v74, 0x3f803f80\n" \
    "v_mov_b32 v75, 0x3f803f80\n" \
    "s_waitcnt lgkmcnt(12)\n" \
    "v_mfma_f32_16x16x32_bf16 v[80:83], v[120:123], v[120:123], 0\n" \
    "v_mfma_f32_16x16x32_bf16 v[84:87], v[72:75], v[120:123], 0\n" \
    "ds_read_b64_tr_b16 v[116:117], %[wa] offset:5120\n" \
    "ds_read_b64_tr_b16 v[118:119], %[wa] offset:5248\n" \
    "ds_read_b128 v[140:143], %[xa] offset:320\n" \
    "v_mfma_f32_16x16x32_bf16 v[64:67], v[96:99], v[120:123], 0\n" \
    "v_mfma_f32_16x16x32_bf16 v[68:71], v[96:99], v[72:75], 0\n" \
    "s_waitcnt lgkmcnt(12)\n" \
    "v_mfma_f32_16x16x32_bf16 v[80:83], v[124:127], v[124:127], v[80:83]\n" \
    "v_mfma_f32_16x16x32_bf16 v[84:87], v[72:75], v[124:127], v[84:87]\n" \
    "ds_read_b64_tr_b16 v[96:97], %[wa] offset:6144\n" \
    "ds_read_b64_tr_b16 v[98:99], %[wa] offset:6272\n" \
    "ds_read_b128 v[120:123], %[xa] offset:384\n" \
    "v_mfma_f32_16x16x32_bf16 v[64:67], v[100:103], v[124:127], v[64:67]\n" \
    "v_mfma_f32_16x16x32_bf16 v[68:71], v[100:103], v[72:75], v[68:71]\n" \
    "s_waitcnt lgkmcnt(12)\n" \
    "v_mfma_f32_16x16x32_bf16 v[80:83], v[128:131], v[128:131], v[80:83]\n" \
    "v_mfma_f32_16x16x32_bf16 v[84:87], v[72:75], v[128:131], v[84:87]\n" \
    "ds_read_b64_tr_b16 v[100:101], %[wa] offset:7168\n" \
    "ds_read_b64_tr_b16 v[102:103], %[wa] offset:7296\n" \
    "ds_read_b128 v[124:127], %[xa] offset:448\n" \
    "v_mfma_f32_16x16x32_bf16 v[64:67], v[104:107], v[128:131], v[64:67]\n" \
    "v_mfma_f32_16x16x32_bf16 v[68:71], v[104:107], v[72:75], v[68:71]\n" \
    "s_waitcnt lgkmcnt(12)\n" \
    "v_mfma_f32_16x16x32_bf16 v[80:83], v[132:135], v[132:135], v[80:83]\n" \
    "v_mfma_f32_16x16x32_bf16 v[84:87], v[72:75], v[132:135], v[84:87]\n" \
    "ds_read_b64_tr_b16 v[104:105], %[wa] offset:8192\n" \
    "ds_read_b64_tr_b16 v[106:107], %[wa] offset:8320\n" \
    "ds_read_b128 v[128:131], %[xa] offset:512\n" \
    "v_mfma_f32_16x16x32_bf16 v[64:67], v[108:111], v[132:135], v[64:67]\n" \
    "v_mfma_f32_16x16x32_bf16 v[68:71], v[108:111], v[72:75], v[68:71]\n" \
    "s_waitcnt lgkmcnt(12)\n" \
    "v_mfma_f32_16x16x32_bf16 v[80:83], v[136:139], v[136:139], v[80:83]\n" \
    "v_mfma_f32_16x16x32_bf16 v[84:87], v[72:75], v[136:139], v[84:87]\n" \
    "ds_read_b64_tr_b16 v[108:109], %[wa] offset:9216\n" \
    "ds_read_b64_tr_b16 v[110:111], %[wa] offset:9344\n" \
    "ds_read_b128 v[132:135], %[xa] offset:576\n" \
    "v_mfma_f32_16x16x32_bf16 v[64:67], v[112:115], v[136:139], v[64:67]\n" \
    "v_mfma_f32_16x16x32_bf16 v[68:71], v[112:115], v[72:75], v[68:71]\n" \
    "s_waitcnt lgkmcnt(12)\n" \
    "v_mfma_f32_16x16x32_bf16 v[80:83], v[140:143], v[140:143], v[80:83]\n" \
    "v_mfma_f32_16x16x32_bf16 v[84:87], v[72:75], v[140:143], v[84:87]\n" \
    "ds_read_b64_tr_b16 v[112:113], %[wa] offset:10240\n" \
    "ds_read_b64_tr_b16 v[114:115], %[wa] offset:10368\n" \
    "ds_read_b128 v[136:139], %[xa] offset:640\n" \
    "v_mfma_f32_16x16x32_bf16 v[64:67], v[116:119], v[140:143], v[64:67]\n" \
    "v_mfma_f32_16x16x32_bf16 v[68:71], v[116:119], v[72:75], v[68:71]\n" \
    "s_waitcnt lgkmcnt(12)\n" \
    "v_mfma_f32_16x16x32_bf16 v[80:83], v[120:123], v[120:123], v[80:83]\n" \
    "v_mfma_f32_16x16x32_bf16 v[84:87], v[72:75], v[120:123], v[84:87]\n" \
    "ds_read_b64_tr_b16 v[116:117], %[wa] offset:11264\n" \
    "ds_read_b64_tr_b16 v[118:119], %[wa] offset:11392\n" \
    "ds_read_b128 v[140:143], %[xa] offset:704\n" \
    "v_mfma_f32_16x16x32_bf16 v[64:67], v[96:99], v[120:123], v[64:67]\n" \
    "v_mfma_f32_16x16x32_bf16 v[68:71], v[96:99], v[72:75], v[68:71]\n" \
    "s_waitcnt lgkmcnt(12)\n" \
    "v_mfma_f32_16x16x32_bf16 v[80:83], v[124:127], v[124:127], v[80:83]\n" \
    "v_mfma_f32_16x16x32_bf16 v[84:87], v[72:75], v[124:127], v[84:87]\n" \
    "ds_read_b64_tr_b16 v[96:97], %[wa] offset:12288\n" \
    "ds_read_b64_tr_b16 v[98:99], %[wa] offset:12416\n" \
    "ds_read_b128 v[120:123], %[xa] offset:768\n" \
    "v_mfma_f32_16x16x32_bf16 v[64:67], v[100:103], v[124:127], v[64:67]\n" \
    "v_mfma_f32_16x16x32_bf16 v[68:71], v[100:103], v[72:75], v[68:71]\n" \
    "s_waitcnt lgkmcnt(12)\n" \
    "v_mfma_f32_16x16x32_bf16 v[80:83], v[128:131], v[128:131], v[80:83]\n" \
    "v_mfma_f32_16x16x32_bf16 v[84:87], v[72:75], v[128:131], v[84:87]\n" \
    "ds_read_b64_tr_b16 v[100:101], %[wa] offset:13312\n" \
    "ds_read_b64_tr_b16 v[102:103], %[wa] offset:13440\n" \
    "ds_read_b128 v[124:127], %[xa] offset:832\n" \
    "v_mfma_f32_16x16x32_bf16 v[64:67], v[104:107], v[128:131], v[64:67]\n" \
    "v_mfma_f32_16x16x32_bf16 v[68:71], v[104:107], v[72:75], v[68:71]\n" \
    "s_waitcnt lgkmcnt(12)\n" \
    "v_mfma_f32_16x16x32_bf16 v[80:83], v[132:135], v[132:135], v[80:83]\n" \
    "v_mfma_f32_16x16x32_bf16 v[84:87], v[72:75], v[132:135], v[84:87]\n" \
    "ds_read_b64_tr_b16 v[104:105], %[wa] offset:14336\n" \
    "ds_read_b64_tr_b16 v[106:107], %[wa] offset:14464\n" \
    "ds_read_b128 v[128:131], %[xa] offset:896\n" \
    "v_mfma_f32_16x16x32_bf16 v[64:67], v[108:111], v[132:135], v[64:67]\n" \
    "v_mfma_f32_16x16x32_bf16 v[68:71], v[108:111], v[72:75], v[68:71]\n" \
    "s_waitcnt lgkmcnt(12)\n" \
    "v_mfma_f32_16x16x32_bf16 v[80:83], v[136:139], v[136:139], v[80:83]\n" \
    "v_mfma_f32_16x16x32_bf16 v[84:87], v[72:75], v[136:139], v[84:87]\n" \
    "ds_read_b64_tr_b16 v[108:109], %[wa] offset:15360\n" \
    "ds_read_b64_tr_b16 v[110:111], %[wa] offset:15488\n" \
    "ds_read_b128 v[132:135], %[xa] offset:960\n" \
    "v_mfma_f32_16x16x32_bf16 v[64:67], v[112:115], v[136:139], v[64:67]\n" \
    "v_mfma_f32_16x16x32_bf16 v[68:71], v[112:115], v[72:75], v[68:71]\n" \
    "s_waitcnt lgkmcnt(12)\n" \
    "v_mfma_f32_16x16x32_bf16 v[80:83], v[140:143], v[140:143], v[80:83]\n" \
    "v_mfma_f32_16x16x32_bf16 v[84:87], v[72:75], v[140:143], v[84:87]\n" \
    "v_mfma_f32_16x16x32_bf16 v[64:67], v[116:119], v[140:143], v[64:67]\n" \
    "v_mfma_f32_16x16x32_bf16 v[68:71], v[116:119], v[72:75], v[68:71]\n" \
    "s_waitcnt lgkmcnt(9)\n" \
    "v_mfma_f32_16x16x32_bf16 v[80:83], v[120:123], v[120:123], v[80:83]\n" \
    "v_mfma_f32_16x16x32_bf16 v[84:87], v[72:75], v[120:123], v[84:87]\n" \
    "v_mfma_f32_16x16x32_bf16 v[64:67], v[96:99], v[120:123], v[64:67]\n" \
    "v_mfma_f32_16x16x32_bf16 v[68:71], v[96:99], v[72:75], v[68:71]\n" \
    "s_waitcnt lgkmcnt(6)\n" \
    "v_mfma_f32_16x16x32_bf16 v[80:83], v[124:127], v[124:127], v[80:83]\n" \
    "v_mfma_f32_16x16x32_bf16 v[84:87], v[72:75], v[124:127], v[84:87]\n" \
    "v_mfma_f32_16x16x32_bf16 v[64:67], v[100:103], v[124:127], v[64:67]\n" \
    "v_mfma_f32_16x16x32_bf16 v[68:71], v[100:103], v[72:75], v[68:71]\n" \
    "s_waitcnt lgkmcnt(3)\n" \
    "v_mfma_f32_16x16x32_bf16 v[80:83], v[128:131], v[128:131], v[80:83]\n" \
    "v_mfma_f32_16x16x32_bf16 v[84:87], v[72:75], v[128:131], v[84:87]\n" \
    "v_mfma_f32_16x16x32_bf16 v[64:67], v[104:107], v[128:131], v[64:67]\n" \
    "v_mfma_f32_16x16x32_bf16 v[68:71], v[104:107], v[72:75], v[68:71]\n" \
    "s_waitcnt lgkmcnt(0)\n" \
    "v_mfma_f32_16x16x32_bf16 v[80:83], v[132:135], v[132:135], v[80:83]\n" \
    "v_mfma_f32_16x16x32_bf16 v[84:87], v[72:75], v[132:135], v[84:87]\n" \
    "v_mfma_f32_16x16x32_bf16 v[64:67], v[108:111], v[132:135], v[64:67]\n" \
    "v_mfma_f32_16x16x32_bf16 v[68:71], v[108:111], v[72:75], v[68:71]\n" \
    "s_nop 4\n" \
    "v_cndmask_b32 v80, v80, v81, %[m1]\n" \
    "v_cndmask_b32 v82, v82, v83, %[m1]\n" \
    "s_nop 0\n" \
    "v_cndmask_b32 v80, v80, v82, %[m2]\n" \
    "s_nop 0\n" \
    "v_cndmask_b32 v85, 0, v80, %[mq]\n" \
    "v_mov_b32 v86, v85\n" \
    "s_nop 1\n" \
    "v_permlane16_swap_b32 v85, v86\n" \
    "v_add_f32 v85, v85, v86\n" \
    "v_mov_b32 v86, v85\n" \
    "s_nop 1\n" \
    "v_permlane32_swap_b32 v85, v86\n" \
    "v_add_f32 v85, v85, v86\n" \
    "v_mul_f32 v76, 0x3b000000, v84\n" \
    "v_mul_f32 v77, 0x3b000000, v85\n" \
    "v_fma_f32 v77, -v76, v76, v77\n" \
    "v_max_f32 v77, 0, v77\n" \
    "v_add_f32 v77, 0x3727c5ac, v77\n" \
    "v_rsq_f32 v78, v77\n" \
    "s_nop 0\n" \
    "v_mul_f32 v79, v76, v78\n" \
    "v_mul_f32 v68, v68, v79\n" \
    "v_mul_f32 v69, v69, v79\n" \
    "v_mul_f32 v70, v70, v79\n" \
    "v_mul_f32 v71, v71, v79\n" \
    "v_fma_f32 v64, v64, v78, -v68\n" \
    "v_fma_f32 v65, v65, v78, -v69\n" \
    "v_fma_f32 v66, v66, v78, -v70\n" \
    "v_fma_f32 v67, v67, v78, -v71\n" \
    "v_cvt_pk_bf16_f32 v64, v64, v65\n" \
    "v_cvt_pk_bf16_f32 v65, v66, v67\n" \
    "s_cmp_eq_u32 %[isv], 0\n" \
    "s_cbranch_scc1 3f\n" \
    "ds_write_b64 %[tw], v[64:65]\n" \
    "s_waitcnt lgkmcnt(0)\n" \
    "ds_read_b64_tr_b16 v[64:65], %[tr]\n" \
    "s_waitcnt lgkmcnt(0)\n" \
    "3:\n" \
    "buffer_store_dwordx2 v[64:65], %[so], %[rs], 0 offen sc1\n" \
    "s_setprio 0\n"
#define KV_CLOB "v64", "v65", "v66", "v67", "v68", "v69", "v70", "v71", "v72", "v73", "v74", "v75", "v76", "v77", "v78", "v79", "v80", "v81", "v82", "v83", "v84", "v85", "v86", "v87", "v96", "v97", "v98", "v99", "v100", "v101", "v102", "v103", "v104", "v105", "v106", "v107", "v108", "v109", "v110", "v111", "v112", "v113", "v114", "v115", "v116", "v117", "v118", "v119", "v120", "v121", "v122", "v123", "v124", "v125", "v126", "v127", "v128", "v129", "v130", "v131", "v132", "v133", "v134", "v135", "v136", "v137", "v138", "v139", "v140", "v141", "v142", "v143"
#define ST_ASM \
    "ds_read_b128 v[96:99], %[xa] offset:0\n" \
    "ds_read_b128 v[100:103], %[xa] offset:32\n" \
    "ds_read_b128 v[104:107], %[xa] offset:64\n" \
    "ds_read_b128 v[108:111], %[xa] offset:96\n" \
    "ds_read_b128 v[112:115], %[xa] offset:128\n" \
    "ds_read_b128 v[116:119], %[xa] offset:160\n" \
    "ds_read_b128 v[120:123], %[xa] offset:192\n" \
    "ds_read_b128 v[124:127], %[xa] offset:224\n" \
    "v_mov_b32 v80, 0x3f803f80\n" \
    "v_mov_b32 v81, 0x3f803f80\n" \
    "s_waitcnt lgkmcnt(7)\n" \
    "v_mfma_f32_4x4x4_16b_bf16 v[72:75], v[96:97], v[96:97], 0\n" \
    "v_mfma_f32_4x4x4_16b_bf16 v[64:67], v[80:81], v[96:97], 0\n" \
    "v_mfma_f32_4x4x4_16b_bf16 v[76:79], v[98:99], v[98:99], 0\n" \
    "v_mfma_f32_4x4x4_16b_bf16 v[68:71], v[80:81], v[98:99], 0\n" \
    "s_waitcnt lgkmcnt(6)\n" \
    "v_mfma_f32_4x4x4_16b_bf16 v[72:75], v[100:101], v[100:101], v[72:75]\n" \
    "v_mfma_f32_4x4x4_16b_bf16 v[64:67], v[80:81], v[100:101], v[64:67]\n" \
    "v_mfma_f32_4x4x4_16b_bf16 v[76:79], v[102:103], v[102:103], v[76:79]\n" \
    "v_mfma_f32_4x4x4_16b_bf16 v[68:71], v[80:81], v[102:103], v[68:71]\n" \
    "s_waitcnt lgkmcnt(5)\n" \
    "v_mfma_f32_4x4x4_16b_bf16 v[72:75], v[104:105], v[104:105], v[72:75]\n" \
    "v_mfma_f32_4x4x4_16b_bf16 v[64:67], v[80:81], v[104:105], v[64:67]\n" \
    "v_mfma_f32_4x4x4_16b_bf16 v[76:79], v[106:107], v[106:107], v[76:79]\n" \
    "v_mfma_f32_4x4x4_16b_bf16 v[68:71], v[80:81], v[106:107], v[68:71]\n" \
    "s_waitcnt lgkmcnt(4)\n" \
    "v_mfma_f32_4x4x4_16b_bf16 v[72:75], v[108:109], v[108:109], v[72:75]\n" \
    "v_mfma_f32_4x4x4_16b_bf16 v[64:67], v[80:81], v[108:109], v[64:67]\n" \
    "v_mfma_f32_4x4x4_16b_bf16 v[76:79], v[110:111], v[110:111], v[76:79]\n" \
    "v_mfma_f32_4x4x4_16b_bf16 v[68:71], v[80:81], v[110:111], v[68:71]\n" \
    "s_waitcnt lgkmcnt(3)\n" \
    "v_mfma_f32_4x4x4_16b_bf16 v[72:75], v[112:113], v[112:113], v[72:75]\n" \
    "v_mfma_f32_4x4x4_16b_bf16 v[64:67], v[80:81], v[112:113], v[64:67]\n" \
    "v_mfma_f32_4x4x4_16b_bf16 v[76:79], v[114:115], v[114:115], v[76:79]\n" \
    "v_mfma_f32_4x4x4_16b_bf16 v[68:71], v[80:81], v[114:115], v[68:71]\n" \
    "s_waitcnt lgkmcnt(2)\n" \
    "v_mfma_f32_4x4x4_16b_bf16 v[72:75], v[116:117], v[116:117], v[72:75]\n" \
    "v_mfma_f32_4x4x4_16b_bf16 v[64:67], v[80:81], v[116:117], v[64:67]\n" \
    "v_mfma_f32_4x4x4_16b_bf16 v[76:79], v[118:119], v[118:119], v[76:79]\n" \
    "v_mfma_f32_4x4x4_16b_bf16 v[68:71], v[80:81], v[118:119], v[68:71]\n" \
    "s_waitcnt lgkmcnt(1)\n" \
    "v_mfma_f32_4x4x4_16b_bf16 v[72:75], v[120:121], v[120:121], v[72:75]\n" \
    "v_mfma_f32_4x4x4_16b_bf16 v[64:67], v[80:81], v[120:121], v[64:67]\n" \
    "v_mfma_f32_4x4x4_16b_bf16 v[76:79], v[122:123], v[122:123], v[76:79]\n" \
    "v_mfma_f32_4x4x4_16b_bf16 v[68:71], v[80:81], v[122:123], v[68:71]\n" \
    "s_waitcnt lgkmcnt(0)\n" \
    "v_mfma_f32_4x4x4_16b_bf16 v[72:75], v[124:125], v[124:125], v[72:75]\n" \
    "v_mfma_f32_4x4x4_16b_bf16 v[64:67], v[80:81], v[124:125], v[64:67]\n" \
    "v_mfma_f32_4x4x4_16b_bf16 v[76:79], v[126:127], v[126:127], v[76:79]\n" \
    "v_mfma_f32_4x4x4_16b_bf16 v[68:71], v[80:81], v[126:127], v[68:71]\n" \
    "s_nop 4\n" \
    "v_pk_add_f32 v[72:73], v[72:73], v[76:77]\n" \
    "v_pk_add_f32 v[74:75], v[74:75], v[78:79]\n" \
    "v_add_f32 v64, v64, v68\n" \
    "v_cndmask_b32 v72, v72, v73, %[m1]\n" \
    "v_cndmask_b32 v74, v74, v75, %[m1]\n" \
    "s_nop 0\n" \
    "v_cndmask_b32 v65, v72, v74, %[m2]\n" \
    "s_nop 0\n" \
    "ds_write_b64 %[sw], v[64:65]\n"
#define ST_CLOB "v64", "v65", "v66", "v67", "v68", "v69", "v70", "v71", "v72", "v73", "v74", "v75", "v76", "v77", "v78", "v79", "v80", "v81", "v82", "v96", "v97", "v98", "v99", "v100", "v101", "v102", "v103", "v104", "v105", "v106", "v107", "v108", "v109", "v110", "v111", "v112", "v113", "v114", "v115", "v116", "v117", "v118", "v119", "v120", "v121", "v122", "v123", "v124", "v125", "v126", "v127"
#define CSP_ASM \
    "s_mov_b32 s61, 0x400000\n" \
    "1:\n" \
    "ds_read_b32 v76, %[cnt]\n" \
    "s_sub_u32 s61, s61, 1\n" \
    "s_cbranch_scc1 2f\n" \
    "s_waitcnt lgkmcnt(0)\n" \
    "v_readfirstlane_b32 s60, v76\n" \
    "s_nop 3\n" \
    "s_cmp_ge_u32 s60, 4\n" \
    "s_cbranch_scc0 1b\n" \
    "2:\n" \
    "ds_read_b64_tr_b16 v[96:97], %[wa] offset:0\n" \
    "ds_read_b64_tr_b16 v[98:99], %[wa] offset:256\n" \
    "ds_read_b64_tr_b16 v[100:101], %[wa] offset:8192\n" \
    "ds_read_b64_tr_b16 v[102:103], %[wa] offset:8448\n" \
    "v_mov_b32 v64, 0\n" \
    "v_mov_b32 v65, 0\n" \
    "ds_read_b64_tr_b16 v[104:105], %[wa] offset:512\n" \
    "ds_read_b64_tr_b16 v[106:107], %[wa] offset:768\n" \
    "ds_read_b64_tr_b16 v[108:109], %[wa] offset:8704\n" \
    "ds_read_b64_tr_b16 v[110:111], %[wa] offset:8960\n" \
    "v_mov_b32 v66, 0\n" \
    "v_mov_b32 v67, 0\n" \
    "ds_read_b64_tr_b16 v[112:113], %[wa] offset:1024\n" \
    "ds_read_b64_tr_b16 v[114:115], %[wa] offset:1280\n" \
    "ds_read_b64_tr_b16 v[116:117], %[wa] offset:9216\n" \
    "ds_read_b64_tr_b16 v[118:119], %[wa] offset:9472\n" \
    "v_mov_b32 v68, 0\n" \
    "v_mov_b32 v69, 0\n" \
    "ds_read_b64_tr_b16 v[120:121], %[wa] offset:1536\n" \
    "ds_read_b64_tr_b16 v[122:123], %[wa] offset:1792\n" \
    "ds_read_b64_tr_b16 v[124:125], %[wa] offset:9728\n" \
    "ds_read_b64_tr_b16 v[126:127], %[wa] offset:9984\n" \
    "v_mov_b32 v70, 0\n" \
    "v_mov_b32 v71, 0\n" \
    "s_waitcnt lgkmcnt(12)\n" \
    "v_dot2c_f32_bf16 v64, %[one], v96\n" \
    "v_dot2c_f32_bf16 v65, %[one], v97\n" \
    "v_dot2c_f32_bf16 v64, %[one], v98\n" \
    "v_dot2c_f32_bf16 v65, %[one], v99\n" \
    "v_dot2c_f32_bf16 v64, %[one], v100\n" \
    "v_dot2c_f32_bf16 v65, %[one], v101\n" \
    "v_dot2c_f32_bf16 v64, %[one], v102\n" \
    "v_dot2c_f32_bf16 v65, %[one], v103\n" \
    "s_waitcnt lgkmcnt(8)\n" \
    "v_dot2c_f32_bf16 v66, %[one], v104\n" \
    "v_dot2c_f32_bf16 v67, %[one], v105\n" \
    "v_dot2c_f32_bf16 v66, %[one], v106\n" \
    "v_dot2c_f32_bf16 v67, %[one], v107\n" \
    "v_dot2c_f32_bf16 v66, %[one], v108\n" \
    "v_dot2c_f32_bf16 v67, %[one], v109\n" \
    "v_dot2c_f32_bf16 v66, %[one], v110\n" \
    "v_dot2c_f32_bf16 v67, %[one], v111\n" \
    "s_waitcnt lgkmcnt(4)\n" \
    "v_dot2c_f32_bf16 v68, %[one], v112\n" \
    "v_dot2c_f32_bf16 v69, %[one], v113\n" \
    "v_dot2c_f32_bf16 v68, %[one], v114\n" \
    "v_dot2c_f32_bf16 v69, %[one], v115\n" \
    "v_dot2c_f32_bf16 v68, %[one], v116\n" \
    "v_dot2c_f32_bf16 v69, %[one], v117\n" \
    "v_dot2c_f32_bf16 v68, %[one], v118\n" \
    "v_dot2c_f32_bf16 v69, %[one], v119\n" \
    "s_waitcnt lgkmcnt(0)\n" \
    "v_dot2c_f32_bf16 v70, %[one], v120\n" \
    "v_dot2c_f32_bf16 v71, %[one], v121\n" \
    "v_dot2c_f32_bf16 v70, %[one], v122\n" \
    "v_dot2c_f32_bf16 v71, %[one], v123\n" \
    "v_dot2c_f32_bf16 v70, %[one], v124\n" \
    "v_dot2c_f32_bf16 v71, %[one], v125\n" \
    "v_dot2c_f32_bf16 v70, %[one], v126\n" \
    "v_dot2c_f32_bf16 v71, %[one], v127\n" \
    "v_mov_b32 v73, %[tag]\n" \
    "v_mov_b32 v75, %[tag]\n" \
    "s_nop 0\n" \
    "v_add_f32 v64, v64, v65\n" \
    "v_add_f32 v66, v66, v67\n" \
    "v_add_f32 v68, v68, v69\n" \
    "v_add_f32 v70, v70, v71\n" \
    "s_nop 0\n" \
    "v_permlane32_swap_b32 v64, v66\n" \
    "v_permlane32_swap_b32 v68, v70\n" \
    "s_movk_i32 s60, 8192\n" \
    "v_add_f32 v72, v64, v66\n" \
    "v_add_f32 v74, v68, v70\n" \
    "buffer_store_dwordx2 v[72:73], %[csp], %[rs], 0 offen sc1\n" \
    "buffer_store_dwordx2 v[74:75], %[csp], %[rs], s60 offen sc1\n"
#define PRE_ASM \
    "s_add_u32 m0, %[ring], 32768\n" \
    "s_nop 0\n" \
    "global_load_lds_dwordx4 %[voA], %[srcA]\n" \
    "v_add_u32 %[voA], %[stepA], %[voA]\n" \
    "v_subrev_u32 v159, %[totA], %[voA]\n" \
    "v_min_u32 %[voA], v159, %[voA]\n" \
    "s_add_u32 m0, %[ring], 33792\n" \
    "s_nop 0\n" \
    "global_load_lds_dwordx4 %[voB], %[srcB]\n" \
    "s_add_u32 m0, %[ring], 34688\n" \
    "s_nop 0\n" \
    "global_load_lds_dwordx4 %[voB], %[srcB] offset:128\n" \
    "v_add_u32 %[voB], %[stepB], %[voB]\n" \
    "v_subrev_u32 v159, %[totB], %[voB]\n" \
    "v_min_u32 %[voB], v159, %[voB]\n" \
    "s_add_u32 m0, %[ring], 35840\n" \
    "s_nop 0\n" \
    "global_load_lds_dwordx4 %[voD], %[srcB]\n" \
    "v_add_u32 %[voD], %[stepB], %[voD]\n" \
    "v_subrev_u32 v159, %[totB], %[voD]\n" \
    "v_min_u32 %[voD], v159, %[voD]\n" \
    "s_add_u32 m0, %[ring], 49152\n" \
    "s_nop 0\n" \
    "global_load_lds_dwordx4 %[voA], %[srcA]\n" \
    "v_add_u32 %[voA], %[stepA], %[voA]\n" \
    "v_subrev_u32 v159, %[totA], %[voA]\n" \
    "v_min_u32 %[voA], v159, %[voA]\n" \
    "s_add_u32 m0, %[ring], 50176\n" \
    "s_nop 0\n" \
    "global_load_lds_dwordx4 %[voB], %[srcB]\n" \
    "s_add_u32 m0, %[ring], 51072\n" \
    "s_nop 0\n" \
    "global_load_lds_dwordx4 %[voB], %[srcB] offset:128\n" \
    "v_add_u32 %[voB], %[stepB], %[voB]\n" \
    "v_subrev_u32 v159, %[totB], %[voB]\n" \
    "v_min_u32 %[voB], v159, %[voB]\n" \
    "s_add_u32 m0, %[ring], 52224\n" \
    "s_nop 0\n" \
    "global_load_lds_dwordx4 %[voD], %[srcB]\n" \
    "v_add_u32 %[voD], %[stepB], %[voD]\n" \
    "v_subrev_u32 v159, %[totB], %[voD]\n" \
    "v_min_u32 %[voD], v159, %[voD]\n" \
    "s_waitcnt vmcnt(12)\n" \
    "v_mov_b32 v159, 1\n" \
    "s_mov_b64 exec, 1\n" \
    "ds_add_u32 %[cnt], v159\n" \
    "s_mov_b64 exec, -1\n"
#define P1L_ASM \
    "s_mov_b32 s61, 0x400000\n" \
    "23:\n" \
    "ds_read_b32 v156, %[cnt]\n" \
    "ds_read_b64_tr_b16 v[96:97], %[wa] offset:0\n" \
    "ds_read_b64_tr_b16 v[98:99], %[wa] offset:256\n" \
    "ds_read_b128 v[100:103], %[xa] offset:0\n" \
    "ds_read_b128 v[104:107], %[xa] offset:33280\n" \
    "ds_read_b64_tr_b16 v[108:109], %[wa] offset:8192\n" \
    "ds_read_b64_tr_b16 v[110:111], %[wa] offset:8448\n" \
    "ds_read_b128 v[112:115], %[xa] offset:32\n" \
    "ds_read_b128 v[116:119], %[xa] offset:33312\n" \
    "s_sub_u32 s61, s61, 1\n" \
    "s_cbranch_scc1 24f\n" \
    "s_waitcnt lgkmcnt(8)\n" \
    "v_readfirstlane_b32 s60, v156\n" \
    "s_nop 3\n" \
    "s_cmp_ge_u32 s60, 4\n" \
    "s_cbranch_scc1 24f\n" \
    "s_waitcnt lgkmcnt(0)\n" \
    "s_branch 23b\n" \
    "24:\n" \
    "s_waitcnt lgkmcnt(0)\n" \
    "s_barrier\n" \
    "v_mfma_f32_32x32x16_bf16 v[64:79], v[96:99], v[100:103], 0\n" \
    "ds_read_b64_tr_b16 v[120:121], %[wa] offset:16384\n" \
    "ds_read_b64_tr_b16 v[122:123], %[wa] offset:16640\n" \
    "ds_read_b64_tr_b16 v[132:133], %[wa] offset:24576\n" \
    "ds_read_b64_tr_b16 v[134:135], %[wa] offset:24832\n" \
    "ds_read_b128 v[124:127], %[xa] offset:64\n" \
    "ds_read_b128 v[128:131], %[xa] offset:33344\n" \
    "ds_read_b128 v[136:139], %[xa] offset:96\n" \
    "ds_read_b128 v[140:143], %[xa] offset:33376\n" \
    "v_mfma_f32_32x32x16_bf16 v[80:95], v[96:99], v[104:107], 0\n" \
    "v_mfma_f32_32x32x16_bf16 v[64:79], v[108:111], v[112:115], v[64:79]\n" \
    "v_mfma_f32_32x32x16_bf16 v[80:95], v[108:111], v[116:119], v[80:95]\n" \
    "s_waitcnt lgkmcnt(0)\n" \
    "s_barrier\n" \
    "v_mfma_f32_32x32x16_bf16 v[64:79], v[120:123], v[124:127], v[64:79]\n" \
    "ds_read_b64_tr_b16 v[96:97], %[wa] offset:32768\n" \
    "ds_read_b64_tr_b16 v[98:99], %[wa] offset:33024\n" \
    "ds_read_b64_tr_b16 v[108:109], %[wa] offset:40960\n" \
    "ds_read_b64_tr_b16 v[110:111], %[wa] offset:41216\n" \
    "ds_read_b128 v[100:103], %[xa] offset:128\n" \
    "ds_read_b128 v[104:107], %[xa] offset:33408\n" \
    "ds_read_b128 v[112:115], %[xa] offset:160\n" \
    "ds_read_b128 v[116:119], %[xa] offset:33440\n" \
    "v_mfma_f32_32x32x16_bf16 v[80:95], v[120:123], v[128:131], v[80:95]\n" \
    "v_mfma_f32_32x32x16_bf16 v[64:79], v[132:135], v[136:139], v[64:79]\n" \
    "ds_read_b128 v[176:179], %[sta] offset:0\n" \
    "ds_read_b128 v[180:183], %[sta] offset:16\n" \
    "ds_read_b128 v[184:187], %[sta] offset:32\n" \
    "ds_read_b128 v[188:191], %[sta] offset:48\n" \
    "v_mfma_f32_32x32x16_bf16 v[80:95], v[132:135], v[140:143], v[80:95]\n" \
    "s_cmp_lg_u32 %[w], 0\n" \
    "s_cbranch_scc1 1f\n" \
    "s_waitcnt vmcnt(0)\n" \
    "s_mov_b64 exec, 1\n" \
    "buffer_store_dword %[tag], off, %[rs], %[fo] sc1\n" \
    "s_mov_b64 exec, -1\n" \
    "1:\n" \
    "s_waitcnt lgkmcnt(0)\n" \
    "s_barrier\n" \
    "v_mfma_f32_32x32x16_bf16 v[64:79], v[96:99], v[100:103], v[64:79]\n" \
    "ds_read_b64_tr_b16 v[120:121], %[wa] offset:49152\n" \
    "ds_read_b64_tr_b16 v[122:123], %[wa] offset:49408\n" \
    "ds_read_b64_tr_b16 v[132:133], %[wa] offset:57344\n" \
    "ds_read_b64_tr_b16 v[134:135], %[wa] offset:57600\n" \
    "ds_read_b128 v[124:127], %[xa] offset:192\n" \
    "ds_read_b128 v[128:131], %[xa] offset:33472\n" \
    "ds_read_b128 v[136:139], %[xa] offset:224\n" \
    "ds_read_b128 v[140:143], %[xa] offset:33504\n" \
    "v_mfma_f32_32x32x16_bf16 v[80:95], v[96:99], v[104:107], v[80:95]\n" \
    "v_add_f32 v176, v176, v184\n" \
    "v_add_f32 v177, v177, v185\n" \
    "v_add_f32 v178, v178, v186\n" \
    "v_add_f32 v179, v179, v187\n" \
    "v_add_f32 v180, v180, v188\n" \
    "v_add_f32 v181, v181, v189\n" \
    "v_add_f32 v182, v182, v190\n" \
    "v_add_f32 v183, v183, v191\n" \
    "v_mfma_f32_32x32x16_bf16 v[64:79], v[108:111], v[112:115], v[64:79]\n" \
    "v_add_f32 v176, v176, v180\n" \
    "v_add_f32 v177, v177, v181\n" \
    "v_add_f32 v178, v178, v182\n" \
    "v_add_f32 v179, v179, v183\n" \
    "v_mfma_f32_32x32x16_bf16 v[80:95], v[108:111], v[116:119], v[80:95]\n" \
    "v_add_f32 v176, v176, v178\n" \
    "v_add_f32 v177, v177, v179\n" \
    "v_mul_f32 v192, 0x3b000000, v176\n" \
    "v_mul_f32 v193, 0x3b000000, v177\n" \
    "s_waitcnt lgkmcnt(0)\n" \
    "s_barrier\n" \
    "v_mfma_f32_32x32x16_bf16 v[64:79], v[120:123], v[124:127], v[64:79]\n" \
    "ds_read_b64_tr_b16 v[96:97], %[wa] offset:0\n" \
    "ds_read_b64_tr_b16 v[98:99], %[wa] offset:256\n" \
    "ds_read_b64_tr_b16 v[108:109], %[wa] offset:8192\n" \
    "ds_read_b64_tr_b16 v[110:111], %[wa] offset:8448\n" \
    "ds_read_b128 v[100:103], %[xa] offset:256\n" \
    "ds_read_b128 v[104:107], %[xa] offset:33536\n" \
    "ds_read_b128 v[112:115], %[xa] offset:288\n" \
    "ds_read_b128 v[116:119], %[xa] offset:33568\n" \
    "v_mfma_f32_32x32x16_bf16 v[80:95], v[120:123], v[128:131], v[80:95]\n" \
    "v_fma_f32 v193, -v192, v192, v193\n" \
    "v_mfma_f32_32x32x16_bf16 v[64:79], v[132:135], v[136:139], v[64:79]\n" \
    "v_max_f32 v193, 0, v193\n" \
    "v_cvt_pk_bf16_f32 v178, v192, v192\n" \
    "v_mfma_f32_32x32x16_bf16 v[80:95], v[132:135], v[140:143], v[80:95]\n" \
    "v_add_f32 v193, 0x3727c5ac, v193\n" \
    "v_and_b32 v178, 0xffff0000, v178\n" \
    "v_rsq_f32 v194, v193\n" \
    "v_sub_f32 v192, v192, v178\n" \
    "s_waitcnt lgkmcnt(0)\n" \
    "s_barrier\n" \
    "v_mfma_f32_32x32x16_bf16 v[64:79], v[96:99], v[100:103], v[64:79]\n" \
    "ds_read_b64_tr_b16 v[120:121], %[wa] offset:16384\n" \
    "ds_read_b64_tr_b16 v[122:123], %[wa] offset:16640\n" \
    "ds_read_b64_tr_b16 v[132:133], %[wa] offset:24576\n" \
    "ds_read_b64_tr_b16 v[134:135], %[wa] offset:24832\n" \
    "ds_read_b128 v[124:127], %[xa] offset:320\n" \
    "ds_read_b128 v[128:131], %[xa] offset:33600\n" \
    "ds_read_b128 v[136:139], %[xa] offset:352\n" \
    "ds_read_b128 v[140:143], %[xa] offset:33632\n" \
    "v_mfma_f32_32x32x16_bf16 v[80:95], v[96:99], v[104:107], v[80:95]\n" \
    "v_mul_f32 v195, v193, v194\n" \
    "v_cvt_pk_bf16_f32 v179, v192, v192\n" \
    "v_mfma_f32_32x32x16_bf16 v[64:79], v[108:111], v[112:115], v[64:79]\n" \
    "v_mov_b32 v193, v195\n" \
    "v_and_b32 v179, 0xffff0000, v179\n" \
    "v_mfma_f32_32x32x16_bf16 v[80:95], v[108:111], v[116:119], v[80:95]\n" \
    "v_cvt_pk_bf16_f32 v188, v193, v193\n" \
    "v_sub_f32 v192, v192, v179\n" \
    "v_and_b32 v188, 0xffff0000, v188\n" \
    "ds_write_b64 %[fxs], v[194:195]\n" \
    "s_waitcnt lgkmcnt(0)\n" \
    "s_barrier\n" \
    "v_mfma_f32_32x32x16_bf16 v[64:79], v[120:123], v[124:127], v[64:79]\n" \
    "ds_read_b64_tr_b16 v[96:97], %[wa] offset:32768\n" \
    "ds_read_b64_tr_b16 v[98:99], %[wa] offset:33024\n" \
    "ds_read_b64_tr_b16 v[108:109], %[wa] offset:40960\n" \
    "ds_read_b64_tr_b16 v[110:111], %[wa] offset:41216\n" \
    "ds_read_b128 v[100:103], %[xa] offset:384\n" \
    "ds_read_b128 v[104:107], %[xa] offset:33664\n" \
    "ds_read_b128 v[112:115], %[xa] offset:416\n" \
    "ds_read_b128 v[116:119], %[xa] offset:33696\n" \
    "v_mfma_f32_32x32x16_bf16 v[80:95], v[120:123], v[128:131], v[80:95]\n" \
    "v_sub_f32 v193, v193, v188\n" \
    "v_mov_b32 v183, 0\n" \
    "v_mov_b32 v186, 0\n" \
    "v_mov_b32 v187, 0\n" \
    "v_mfma_f32_32x32x16_bf16 v[64:79], v[132:135], v[136:139], v[64:79]\n" \
    "v_cvt_pk_bf16_f32 v189, v193, v193\n" \
    "v_cvt_pk_bf16_f32 v180, -v178, -v179\n" \
    "v_cvt_pk_bf16_f32 v181, -v178, -v192\n" \
    "v_cvt_pk_bf16_f32 v182, -v179, -v178\n" \
    "v_mfma_f32_32x32x16_bf16 v[80:95], v[132:135], v[140:143], v[80:95]\n" \
    "v_and_b32 v189, 0xffff0000, v189\n" \
    "ds_write_b128 %[fxe], v[180:183]\n" \
    "v_sub_f32 v193, v193, v189\n" \
    "s_waitcnt lgkmcnt(0)\n" \
    "s_barrier\n" \
    "v_mfma_f32_32x32x16_bf16 v[64:79], v[96:99], v[100:103], v[64:79]\n" \
    "ds_read_b64_tr_b16 v[120:121], %[wa] offset:49152\n" \
    "ds_read_b64_tr_b16 v[122:123], %[wa] offset:49408\n" \
    "ds_read_b64_tr_b16 v[132:133], %[wa] offset:57344\n" \
    "ds_read_b64_tr_b16 v[134:135], %[wa] offset:57600\n" \
    "ds_read_b128 v[124:127], %[xa] offset:448\n" \
    "ds_read_b128 v[128:131], %[xa] offset:33728\n" \
    "ds_read_b128 v[136:139], %[xa] offset:480\n" \
    "ds_read_b128 v[140:143], %[xa] offset:33760\n" \
    "v_mfma_f32_32x32x16_bf16 v[80:95], v[96:99], v[104:107], v[80:95]\n" \
    "v_cvt_pk_bf16_f32 v184, v188, v189\n" \
    "v_cvt_pk_bf16_f32 v185, v193, v183\n" \
    "v_mfma_f32_32x32x16_bf16 v[64:79], v[108:111], v[112:115], v[64:79]\n" \
    "ds_write_b128 %[fxe], v[184:187] offset:512\n" \
    "v_mfma_f32_32x32x16_bf16 v[80:95], v[108:111], v[116:119], v[80:95]\n" \
    "s_waitcnt lgkmcnt(0)\n" \
    "s_barrier\n" \
    "v_mfma_f32_32x32x16_bf16 v[64:79], v[120:123], v[124:127], v[64:79]\n" \
    "ds_read_b64_tr_b16 v[96:97], %[wa] offset:0\n" \
    "ds_read_b64_tr_b16 v[98:99], %[wa] offset:256\n" \
    "ds_read_b64_tr_b16 v[108:109], %[wa] offset:8192\n" \
    "ds_read_b64_tr_b16 v[110:111], %[wa] offset:8448\n" \
    "ds_read_b128 v[100:103], %[xa] offset:512\n" \
    "ds_read_b128 v[104:107], %[xa] offset:33792\n" \
    "ds_read_b128 v[112:115], %[xa] offset:544\n" \
    "ds_read_b128 v[116:119], %[xa] offset:33824\n" \
    "v_mfma_f32_32x32x16_bf16 v[80:95], v[120:123], v[128:131], v[80:95]\n" \
    "v_mfma_f32_32x32x16_bf16 v[64:79], v[132:135], v[136:139], v[64:79]\n" \
    "v_mfma_f32_32x32x16_bf16 v[80:95], v[132:135], v[140:143], v[80:95]\n" \
    "s_waitcnt lgkmcnt(0)\n" \
    "s_barrier\n" \
    "v_mfma_f32_32x32x16_bf16 v[64:79], v[96:99], v[100:103], v[64:79]\n" \
    "ds_read_b64_tr_b16 v[120:121], %[wa] offset:16384\n" \
    "ds_read_b64_tr_b16 v[122:123], %[wa] offset:16640\n" \
    "ds_read_b64_tr_b16 v[132:133], %[wa] offset:24576\n" \
    "ds_read_b64_tr_b16 v[134:135], %[wa] offset:24832\n" \
    "ds_read_b128 v[124:127], %[xa] offset:576\n" \
    "ds_read_b128 v[128:131], %[xa] offset:33856\n" \
    "ds_read_b128 v[136:139], %[xa] offset:608\n" \
    "ds_read_b128 v[140:143], %[xa] offset:33888\n" \
    "v_mfma_f32_32x32x16_bf16 v[80:95], v[96:99], v[104:107], v[80:95]\n" \
    "v_mfma_f32_32x32x16_bf16 v[64:79], v[108:111], v[112:115], v[64:79]\n" \
    "v_mfma_f32_32x32x16_bf16 v[80:95], v[108:111], v[116:119], v[80:95]\n" \
    "s_waitcnt lgkmcnt(0)\n" \
    "s_barrier\n" \
    "v_mfma_f32_32x32x16_bf16 v[64:79], v[120:123], v[124:127], v[64:79]\n" \
    "ds_read_b64_tr_b16 v[96:97], %[wa] offset:32768\n" \
    "ds_read_b64_tr_b16 v[98:99], %[wa] offset:33024\n" \
    "ds_read_b64_tr_b16 v[108:109], %[wa] offset:40960\n" \
    "ds_read_b64_tr_b16 v[110:111], %[wa] offset:41216\n" \
    "ds_read_b128 v[100:103], %[xa] offset:640\n" \
    "ds_read_b128 v[104:107], %[xa] offset:33920\n" \
    "ds_read_b128 v[112:115], %[xa] offset:672\n" \
    "ds_read_b128 v[116:119], %[xa] offset:33952\n" \
    "v_mfma_f32_32x32x16_bf16 v[80:95], v[120:123], v[128:131], v[80:95]\n" \
    "v_mfma_f32_32x32x16_bf16 v[64:79], v[132:135], v[136:139], v[64:79]\n" \
    "v_mfma_f32_32x32x16_bf16 v[80:95], v[132:135], v[140:143], v[80:95]\n" \
    "s_waitcnt lgkmcnt(0)\n" \
    "s_barrier\n" \
    "v_mfma_f32_32x32x16_bf16 v[64:79], v[96:99], v[100:103], v[64:79]\n" \
    "ds_read_b64_tr_b16 v[120:121], %[wa] offset:49152\n" \
    "ds_read_b64_tr_b16 v[122:123], %[wa] offset:49408\n" \
    "ds_read_b64_tr_b16 v[132:133], %[wa] offset:57344\n" \
    "ds_read_b64_tr_b16 v[134:135], %[wa] offset:57600\n" \
    "ds_read_b128 v[124:127], %[xa] offset:704\n" \
    "ds_read_b128 v[128:131], %[xa] offset:33984\n" \
    "ds_read_b128 v[136:139], %[xa] offset:736\n" \
    "ds_read_b128 v[140:143], %[xa] offset:34016\n" \
    "v_mfma_f32_32x32x16_bf16 v[80:95], v[96:99], v[104:107], v[80:95]\n" \
    "v_mfma_f32_32x32x16_bf16 v[64:79], v[108:111], v[112:115], v[64:79]\n" \
    "v_mfma_f32_32x32x16_bf16 v[80:95], v[108:111], v[116:119], v[80:95]\n" \
    "s_waitcnt lgkmcnt(0)\n" \
    "s_barrier\n" \
    "v_mfma_f32_32x32x16_bf16 v[64:79], v[120:123], v[124:127], v[64:79]\n" \
    "ds_read_b64_tr_b16 v[96:97], %[wa] offset:0\n" \
    "ds_read_b64_tr_b16 v[98:99], %[wa] offset:256\n" \
    "ds_read_b64_tr_b16 v[108:109], %[wa] offset:8192\n" \
    "ds_read_b64_tr_b16 v[110:111], %[wa] offset:8448\n" \
    "ds_read_b128 v[100:103], %[xa] offset:768\n" \
    "ds_read_b128 v[104:107], %[xa] offset:34048\n" \
    "ds_read_b128 v[112:115], %[xa] offset:800\n" \
    "ds_read_b128 v[116:119], %[xa] offset:34080\n" \
    "v_mfma_f32_32x32x16_bf16 v[80:95], v[120:123], v[128:131], v[80:95]\n" \
    "v_mfma_f32_32x32x16_bf16 v[64:79], v[132:135], v[136:139], v[64:79]\n" \
    "v_mfma_f32_32x32x16_bf16 v[80:95], v[132:135], v[140:143], v[80:95]\n" \
    "s_waitcnt lgkmcnt(0)\n" \
    "s_barrier\n" \
    "buffer_load_dwordx4 v[192:195], %[flo], %[rs], 0 offen sc1\n" \
    "v_mfma_f32_32x32x16_bf16 v[64:79], v[96:99], v[100:103], v[64:79]\n" \
    "ds_read_b64_tr_b16 v[120:121], %[wa] offset:16384\n" \
    "ds_read_b64_tr_b16 v[122:123], %[wa] offset:16640\n" \
    "ds_read_b64_tr_b16 v[132:133], %[wa] offset:24576\n" \
    "ds_read_b64_tr_b16 v[134:135], %[wa] offset:24832\n" \
    "ds_read_b128 v[124:127], %[xa] offset:832\n" \
    "ds_read_b128 v[128:131], %[xa] offset:34112\n" \
    "ds_read_b128 v[136:139], %[xa] offset:864\n" \
    "ds_read_b128 v[140:143], %[xa] offset:34144\n" \
    "buffer_load_dwordx4 v[160:163], %[cso], %[rs], 0 offen offset:0 sc1\n" \
    "v_mfma_f32_32x32x16_bf16 v[80:95], v[96:99], v[104:107], v[80:95]\n" \
    "buffer_load_dwordx4 v[164:167], %[cso], %[rs], 0 offen offset:1024 sc1\n" \
    "v_mfma_f32_32x32x16_bf16 v[64:79], v[108:111], v[112:115], v[64:79]\n" \
    "buffer_load_dwordx4 v[168:171], %[cso], %[rs], 0 offen offset:2048 sc1\n" \
    "v_mfma_f32_32x32x16_bf16 v[80:95], v[108:111], v[116:119], v[80:95]\n" \
    "buffer_load_dwordx4 v[172:175], %[cso], %[rs], 0 offen offset:3072 sc1\n" \
    "s_waitcnt lgkmcnt(0)\n" \
    "s_barrier\n" \
    "v_mfma_f32_32x32x16_bf16 v[64:79], v[120:123], v[124:127], v[64:79]\n" \
    "ds_read_b64_tr_b16 v[96:97], %[wa] offset:32768\n" \
    "ds_read_b64_tr_b16 v[98:99], %[wa] offset:33024\n" \
    "ds_read_b64_tr_b16 v[108:109], %[wa] offset:40960\n" \
    "ds_read_b64_tr_b16 v[110:111], %[wa] offset:41216\n" \
    "ds_read_b128 v[100:103], %[xa] offset:896\n" \
    "ds_read_b128 v[104:107], %[xa] offset:34176\n" \
    "ds_read_b128 v[112:115], %[xa] offset:928\n" \
    "ds_read_b128 v[116:119], %[xa] offset:34208\n" \
    "v_mfma_f32_32x32x16_bf16 v[80:95], v[120:123], v[128:131], v[80:95]\n" \
    "v_mfma_f32_32x32x16_bf16 v[64:79], v[132:135], v[136:139], v[64:79]\n" \
    "ds_read_b128 v[144:147], %[xe]\n" \
    "ds_read_b128 v[148:151], %[xe] offset:1024\n" \
    "ds_read_b64 v[152:153], %[xs]\n" \
    "ds_read_b64 v[154:155], %[xs] offset:256\n" \
    "ds_read_b64 %[xr0], %[xr]\n" \
    "ds_read_b64 %[xr1], %[xr] offset:16\n" \
    "v_mfma_f32_32x32x16_bf16 v[80:95], v[132:135], v[140:143], v[80:95]\n" \
    "s_waitcnt lgkmcnt(0)\n" \
    "s_barrier\n" \
    "v_mfma_f32_32x32x16_bf16 v[64:79], v[96:99], v[100:103], v[64:79]\n" \
    "ds_read_b64_tr_b16 v[120:121], %[wa] offset:49152\n" \
    "ds_read_b64_tr_b16 v[122:123], %[wa] offset:49408\n" \
    "ds_read_b64_tr_b16 v[132:133], %[wa] offset:57344\n" \
    "ds_read_b64_tr_b16 v[134:135], %[wa] offset:57600\n" \
    "ds_read_b128 v[124:127], %[xa] offset:960\n" \
    "ds_read_b128 v[128:131], %[xa] offset:34240\n" \
    "ds_read_b128 v[136:139], %[xa] offset:992\n" \
    "ds_read_b128 v[140:143], %[xa] offset:34272\n" \
    "v_mfma_f32_32x32x16_bf16 v[80:95], v[96:99], v[104:107], v[80:95]\n" \
    "v_mfma_f32_32x32x16_bf16 v[64:79], v[108:111], v[112:115], v[64:79]\n" \
    "v_mov_b32 v223, 0\n" \
    "s_movk_i32 s60, 4096\n" \
    "s_mov_b32 s61, 65536\n" \
    "v_mfma_f32_32x32x16_bf16 v[80:95], v[108:111], v[116:119], v[80:95]\n" \
    "s_waitcnt lgkmcnt(0)\n" \
    "v_mfma_f32_32x32x16_bf16 v[64:79], v[120:123], v[124:127], v[64:79]\n" \
    "v_mfma_f32_32x32x16_bf16 v[80:95], v[120:123], v[128:131], v[80:95]\n" \
    "v_mfma_f32_32x32x16_bf16 v[64:79], v[132:135], v[136:139], v[64:79]\n" \
    "s_waitcnt vmcnt(0)\n" \
    "v_add_f32 v160, v160, v168\n" \
    "v_add_f32 v162, v162, v170\n" \
    "v_add_f32 v164, v164, v172\n" \
    "v_add_f32 v166, v166, v174\n" \
    "v_xor_b32 v161, %[tag], v161\n" \
    "v_xor_b32 v163, %[tag], v163\n" \
    "v_xor_b32 v165, %[tag], v165\n" \
    "v_xor_b32 v167, %[tag], v167\n" \
    "v_xor_b32 v169, %[tag], v169\n" \
    "v_xor_b32 v171, %[tag], v171\n" \
    "v_xor_b32 v173, %[tag], v173\n" \
    "v_xor_b32 v175, %[tag], v175\n" \
    "v_xor_b32 v192, %[tag], v192\n" \
    "v_xor_b32 v193, %[tag], v193\n" \
    "v_xor_b32 v194, %[tag], v194\n" \
    "v_xor_b32 v195, %[tag], v195\n" \
    "v_mfma_f32_32x32x16_bf16 v[80:95], v[132:135], v[140:143], v[80:95]\n" \
    "v_add_f32 v160, v160, v164\n" \
    "v_add_f32 v162, v162, v166\n" \
    "v_or3_b32 v161, v161, v163, v165\n" \
    "v_or3_b32 v167, v167, v169, v171\n" \
    "v_or3_b32 v192, v192, v193, v194\n" \
    "v_add_f32 v156, v160, v162\n" \
    "v_add_f32 v157, v160, v162\n" \
    "v_or3_b32 v161, v161, v173, v175\n" \
    "v_or_b32 v192, v192, v195\n" \
    "v_permlane32_swap_b32 v156, v157\n" \
    "v_or_b32 v161, v161, v167\n" \
    "v_cmp_ne_u32 %[kvbad], 0, v192\n" \
    "v_add_f32 v156, v156, v157\n" \
    "v_cmp_ne_u32 %[csbad], 0, v161\n" \
    "v_cvt_pk_bf16_f32 v157, v156, v156\n" \
    "v_and_b32 v157, 0xffff0000, v157\n" \
    "v_sub_f32 v156, v156, v157\n" \
    "v_cvt_pk_bf16_f32 v158, v156, v156\n" \
    "v_and_b32 v158, 0xffff0000, v158\n" \
    "v_sub_f32 v156, v156, v158\n" \
    "v_cvt_pk_bf16_f32 v220, v157, v157\n" \
    "v_cvt_pk_bf16_f32 v221, v158, v157\n" \
    "v_cvt_pk_bf16_f32 v222, v158, v156\n" \
    "s_mov_b32 exec_lo, 0\n" \
    "v_mov_b32 v220, %[bb]\n" \
    "v_and_b32 v221, 0xffff, %[bb]\n" \
    "v_mov_b32 v222, 0\n" \
    "s_mov_b32 exec_lo, -1\n" \
    "s_cmp_lg_u64 %[csbad], 0\n" \
    "s_cbranch_scc1 10f\n" \
    "v_mfma_f32_32x32x16_bf16 v[64:79], v[220:223], v[144:147], v[64:79]\n" \
    "v_mfma_f32_32x32x16_bf16 v[80:95], v[220:223], v[148:151], v[80:95]\n" \
    "10:\n" \
    "s_cmp_lg_u64 %[kvbad], 0\n" \
    "s_cbranch_scc1 9f\n" \
    "buffer_load_dwordx4 v[196:199], %[kvg], %[rs], 0 offen sc1\n" \
    "buffer_load_dwordx4 v[200:203], %[kvg], %[rs], s61 offen sc1\n" \
    "s_add_u32 s61, s61, s60\n" \
    "buffer_load_dwordx4 v[204:207], %[kvg], %[rs], s60 offen sc1\n" \
    "buffer_load_dwordx4 v[208:211], %[kvg], %[rs], s61 offen sc1\n" \
    "s_add_u32 s61, s61, s60\n" \
    "s_add_u32 s60, s60, s60\n" \
    "buffer_load_dwordx4 v[212:215], %[kvg], %[rs], s60 offen sc1\n" \
    "buffer_load_dwordx4 v[216:219], %[kvg], %[rs], s61 offen sc1\n" \
    "9:\n"
#define P1H_ASM \
    "s_mov_b32 s61, 0x400000\n" \
    "23:\n" \
    "ds_read_b32 v156, %[cnt]\n" \
    "ds_read_b64_tr_b16 v[96:97], %[wa] offset:0\n" \
    "ds_read_b64_tr_b16 v[98:99], %[wa] offset:256\n" \
    "ds_read_b128 v[100:103], %[xa] offset:0\n" \
    "ds_read_b128 v[104:107], %[xa] offset:33280\n" \
    "ds_read_b64_tr_b16 v[108:109], %[wa] offset:8192\n" \
    "ds_read_b64_tr_b16 v[110:111], %[wa] offset:8448\n" \
    "ds_read_b128 v[112:115], %[xa] offset:32\n" \
    "ds_read_b128 v[116:119], %[xa] offset:33312\n" \
    "s_sub_u32 s61, s61, 1\n" \
    "s_cbranch_scc1 24f\n" \
    "s_waitcnt lgkmcnt(8)\n" \
    "v_readfirstlane_b32 s60, v156\n" \
    "s_nop 3\n" \
    "s_cmp_ge_u32 s60, 4\n" \
    "s_cbranch_scc1 24f\n" \
    "s_waitcnt lgkmcnt(0)\n" \
    "s_branch 23b\n" \
    "24:\n" \
    "v_mov_b32 v64, 0\n" \
    "v_mov_b32 v65, 0\n" \
    "v_mov_b32 v66, 0\n" \
    "v_mov_b32 v67, 0\n" \
    "v_mov_b32 v68, 0\n" \
    "v_mov_b32 v69, 0\n" \
    "v_mov_b32 v70, 0\n" \
    "v_mov_b32 v71, 0\n" \
    "v_mov_b32 v72, 0\n" \
    "v_mov_b32 v73, 0\n" \
    "v_mov_b32 v74, 0\n" \
    "v_mov_b32 v75, 0\n" \
    "v_mov_b32 v76, 0\n" \
    "v_mov_b32 v77, 0\n" \
    "v_mov_b32 v78, 0\n" \
    "v_mov_b32 v79, 0\n" \
    "v_mov_b32 v80, 0\n" \
    "v_mov_b32 v81, 0\n" \
    "v_mov_b32 v82, 0\n" \
    "v_mov_b32 v83, 0\n" \
    "v_mov_b32 v84, 0\n" \
    "v_mov_b32 v85, 0\n" \
    "v_mov_b32 v86, 0\n" \
    "v_mov_b32 v87, 0\n" \
    "v_mov_b32 v88, 0\n" \
    "v_mov_b32 v89, 0\n" \
    "v_mov_b32 v90, 0\n" \
    "v_mov_b32 v91, 0\n" \
    "v_mov_b32 v92, 0\n" \
    "v_mov_b32 v93, 0\n" \
    "v_mov_b32 v94, 0\n" \
    "v_mov_b32 v95, 0\n" \
    "s_mov_b32 s61, 3\n" \
    "20:\n" \
    "s_waitcnt vmcnt(8)\n" \
    "s_waitcnt lgkmcnt(0)\n" \
    "s_barrier\n" \
    "s_add_u32 m0, %[ring], 0\n" \
    "s_nop 0\n" \
    "global_load_lds_dwordx4 %[voA], %[srcA]\n" \
    "v_add_u32 %[voA], %[stepA], %[voA]\n" \
    "v_subrev_u32 v159, %[totA], %[voA]\n" \
    "v_min_u32 %[voA], v159, %[voA]\n" \
    "s_add_u32 m0, %[ring], 1024\n" \
    "s_nop 0\n" \
    "global_load_lds_dwordx4 %[voB], %[srcB]\n" \
    "v_mfma_f32_32x32x16_bf16 v[64:79], v[96:99], v[100:103], v[64:79]\n" \
    "ds_read_b64_tr_b16 v[120:121], %[wa] offset:16384\n" \
    "ds_read_b64_tr_b16 v[122:123], %[wa] offset:16640\n" \
    "ds_read_b64_tr_b16 v[132:133], %[wa] offset:24576\n" \
    "ds_read_b64_tr_b16 v[134:135], %[wa] offset:24832\n" \
    "ds_read_b128 v[124:127], %[xa] offset:64\n" \
    "ds_read_b128 v[128:131], %[xa] offset:33344\n" \
    "ds_read_b128 v[136:139], %[xa] offset:96\n" \
    "ds_read_b128 v[140:143], %[xa] offset:33376\n" \
    "v_mfma_f32_32x32x16_bf16 v[80:95], v[96:99], v[104:107], v[80:95]\n" \
    "s_add_u32 m0, %[ring], 1920\n" \
    "s_nop 0\n" \
    "global_load_lds_dwordx4 %[voB], %[srcB] offset:128\n" \
    "v_add_u32 %[voB], %[stepB], %[voB]\n" \
    "v_subrev_u32 v159, %[totB], %[voB]\n" \
    "v_min_u32 %[voB], v159, %[voB]\n" \
    "v_mfma_f32_32x32x16_bf16 v[64:79], v[108:111], v[112:115], v[64:79]\n" \
    "s_add_u32 m0, %[ring], 3072\n" \
    "s_nop 0\n" \
    "global_load_lds_dwordx4 %[voD], %[srcB]\n" \
    "v_add_u32 %[voD], %[stepB], %[voD]\n" \
    "v_subrev_u32 v159, %[totB], %[voD]\n" \
    "v_min_u32 %[voD], v159, %[voD]\n" \
    "v_mfma_f32_32x32x16_bf16 v[80:95], v[108:111], v[116:119], v[80:95]\n" \
    "s_waitcnt vmcnt(8)\n" \
    "s_waitcnt lgkmcnt(0)\n" \
    "s_barrier\n" \
    "s_add_u32 m0, %[ring], 16384\n" \
    "s_nop 0\n" \
    "global_load_lds_dwordx4 %[voA], %[srcA]\n" \
    "v_add_u32 %[voA], %[stepA], %[voA]\n" \
    "v_subrev_u32 v159, %[totA], %[voA]\n" \
    "v_min_u32 %[voA], v159, %[voA]\n" \
    "s_add_u32 m0, %[ring], 17408\n" \
    "s_nop 0\n" \
    "global_load_lds_dwordx4 %[voB], %[srcB]\n" \
    "v_mfma_f32_32x32x16_bf16 v[64:79], v[120:123], v[124:127], v[64:79]\n" \
    "ds_read_b64_tr_b16 v[96:97], %[wa] offset:32768\n" \
    "ds_read_b64_tr_b16 v[98:99], %[wa] offset:33024\n" \
    "ds_read_b64_tr_b16 v[108:109], %[wa] offset:40960\n" \
    "ds_read_b64_tr_b16 v[110:111], %[wa] offset:41216\n" \
    "ds_read_b128 v[100:103], %[xa] offset:128\n" \
    "ds_read_b128 v[104:107], %[xa] offset:33408\n" \
    "ds_read_b128 v[112:115], %[xa] offset:160\n" \
    "ds_read_b128 v[116:119], %[xa] offset:33440\n" \
    "v_mfma_f32_32x32x16_bf16 v[80:95], v[120:123], v[128:131], v[80:95]\n" \
    "s_add_u32 m0, %[ring], 18304\n" \
    "s_nop 0\n" \
    "global_load_lds_dwordx4 %[voB], %[srcB] offset:128\n" \
    "v_add_u32 %[voB], %[stepB], %[voB]\n" \
    "v_subrev_u32 v159, %[totB], %[voB]\n" \
    "v_min_u32 %[voB], v159, %[voB]\n" \
    "v_mfma_f32_32x32x16_bf16 v[64:79], v[132:135], v[136:139], v[64:79]\n" \
    "s_add_u32 m0, %[ring], 19456\n" \
    "s_nop 0\n" \
    "global_load_lds_dwordx4 %[voD], %[srcB]\n" \
    "v_add_u32 %[voD], %[stepB], %[voD]\n" \
    "v_subrev_u32 v159, %[totB], %[voD]\n" \
    "v_min_u32 %[voD], v159, %[voD]\n" \
    "v_mfma_f32_32x32x16_bf16 v[80:95], v[132:135], v[140:143], v[80:95]\n" \
    "s_waitcnt vmcnt(8)\n" \
    "s_waitcnt lgkmcnt(0)\n" \
    "s_barrier\n" \
    "s_add_u32 m0, %[ring], 32768\n" \
    "s_nop 0\n" \
    "global_load_lds_dwordx4 %[voA], %[srcA]\n" \
    "v_add_u32 %[voA], %[stepA], %[voA]\n" \
    "v_subrev_u32 v159, %[totA], %[voA]\n" \
    "v_min_u32 %[voA], v159, %[voA]\n" \
    "s_add_u32 m0, %[ring], 33792\n" \
    "s_nop 0\n" \
    "global_load_lds_dwordx4 %[voB], %[srcB]\n" \
    "v_mfma_f32_32x32x16_bf16 v[64:79], v[96:99], v[100:103], v[64:79]\n" \
    "ds_read_b64_tr_b16 v[120:121], %[wa] offset:49152\n" \
    "ds_read_b64_tr_b16 v[122:123], %[wa] offset:49408\n" \
    "ds_read_b64_tr_b16 v[132:133], %[wa] offset:57344\n" \
    "ds_read_b64_tr_b16 v[134:135], %[wa] offset:57600\n" \
    "ds_read_b128 v[124:127], %[xa] offset:192\n" \
    "ds_read_b128 v[128:131], %[xa] offset:33472\n" \
    "ds_read_b128 v[136:139], %[xa] offset:224\n" \
    "ds_read_b128 v[140:143], %[xa] offset:33504\n" \
    "v_mfma_f32_32x32x16_bf16 v[80:95], v[96:99], v[104:107], v[80:95]\n" \
    "s_add_u32 m0, %[ring], 34688\n" \
    "s_nop 0\n" \
    "global_load_lds_dwordx4 %[voB], %[srcB] offset:128\n" \
    "v_add_u32 %[voB], %[stepB], %[voB]\n" \
    "v_subrev_u32 v159, %[totB], %[voB]\n" \
    "v_min_u32 %[voB], v159, %[voB]\n" \
    "v_mfma_f32_32x32x16_bf16 v[64:79], v[108:111], v[112:115], v[64:79]\n" \
    "s_add_u32 m0, %[ring], 35840\n" \
    "s_nop 0\n" \
    "global_load_lds_dwordx4 %[voD], %[srcB]\n" \
    "v_add_u32 %[voD], %[stepB], %[voD]\n" \
    "v_subrev_u32 v159, %[totB], %[voD]\n" \
    "v_min_u32 %[voD], v159, %[voD]\n" \
    "v_mfma_f32_32x32x16_bf16 v[80:95], v[108:111], v[116:119], v[80:95]\n" \
    "s_waitcnt vmcnt(8)\n" \
    "s_waitcnt lgkmcnt(0)\n" \
    "s_barrier\n" \
    "s_add_u32 m0, %[ring], 49152\n" \
    "s_nop 0\n" \
    "global_load_lds_dwordx4 %[voA], %[srcA]\n" \
    "v_add_u32 %[voA], %[stepA], %[voA]\n" \
    "v_subrev_u32 v159, %[totA], %[voA]\n" \
    "v_min_u32 %[voA], v159, %[voA]\n" \
    "s_add_u32 m0, %[ring], 50176\n" \
    "s_nop 0\n" \
    "global_load_lds_dwordx4 %[voB], %[srcB]\n" \
    "v_mfma_f32_32x32x16_bf16 v[64:79], v[120:123], v[124:127], v[64:79]\n" \
    "ds_read_b64_tr_b16 v[96:97], %[wa] offset:0\n" \
    "ds_read_b64_tr_b16 v[98:99], %[wa] offset:256\n" \
    "ds_read_b64_tr_b16 v[108:109], %[wa] offset:8192\n" \
    "ds_read_b64_tr_b16 v[110:111], %[wa] offset:8448\n" \
    "ds_read_b128 v[100:103], %[xa] offset:256\n" \
    "ds_read_b128 v[104:107], %[xa] offset:33536\n" \
    "ds_read_b128 v[112:115], %[xa] offset:288\n" \
    "ds_read_b128 v[116:119], %[xa] offset:33568\n" \
    "v_mfma_f32_32x32x16_bf16 v[80:95], v[120:123], v[128:131], v[80:95]\n" \
    "s_add_u32 m0, %[ring], 51072\n" \
    "s_nop 0\n" \
    "global_load_lds_dwordx4 %[voB], %[srcB] offset:128\n" \
    "v_add_u32 %[voB], %[stepB], %[voB]\n" \
    "v_subrev_u32 v159, %[totB], %[voB]\n" \
    "v_min_u32 %[voB], v159, %[voB]\n" \
    "v_mfma_f32_32x32x16_bf16 v[64:79], v[132:135], v[136:139], v[64:79]\n" \
    "s_add_u32 m0, %[ring], 52224\n" \
    "s_nop 0\n" \
    "global_load_lds_dwordx4 %[voD], %[srcB]\n" \
    "v_add_u32 %[voD], %[stepB], %[voD]\n" \
    "v_subrev_u32 v159, %[totB], %[voD]\n" \
    "v_min_u32 %[voD], v159, %[voD]\n" \
    "v_mfma_f32_32x32x16_bf16 v[80:95], v[132:135], v[140:143], v[80:95]\n" \
    "v_add_u32 %[xa], 256, %[xa]\n" \
    "s_sub_u32 s61, s61, 1\n" \
    "s_cmp_lg_u32 s61, 0\n" \
    "s_cbranch_scc1 20b\n" \
    "v_subrev_u32 %[xa], 768, %[xa]\n" \
    "s_waitcnt vmcnt(8)\n" \
    "s_waitcnt lgkmcnt(0)\n" \
    "s_barrier\n" \
    "buffer_load_dwordx4 v[192:195], %[flo], %[rs], 0 offen sc1\n" \
    "v_mfma_f32_32x32x16_bf16 v[64:79], v[96:99], v[100:103], v[64:79]\n" \
    "ds_read_b64_tr_b16 v[120:121], %[wa] offset:16384\n" \
    "ds_read_b64_tr_b16 v[122:123], %[wa] offset:16640\n" \
    "ds_read_b64_tr_b16 v[132:133], %[wa] offset:24576\n" \
    "ds_read_b64_tr_b16 v[134:135], %[wa] offset:24832\n" \
    "ds_read_b128 v[124:127], %[xa] offset:832\n" \
    "ds_read_b128 v[128:131], %[xa] offset:34112\n" \
    "ds_read_b128 v[136:139], %[xa] offset:864\n" \
    "ds_read_b128 v[140:143], %[xa] offset:34144\n" \
    "buffer_load_dwordx4 v[160:163], %[cso], %[rs], 0 offen offset:0 sc1\n" \
    "v_mfma_f32_32x32x16_bf16 v[80:95], v[96:99], v[104:107], v[80:95]\n" \
    "buffer_load_dwordx4 v[164:167], %[cso], %[rs], 0 offen offset:1024 sc1\n" \
    "v_mfma_f32_32x32x16_bf16 v[64:79], v[108:111], v[112:115], v[64:79]\n" \
    "buffer_load_dwordx4 v[168:171], %[cso], %[rs], 0 offen offset:2048 sc1\n" \
    "v_mfma_f32_32x32x16_bf16 v[80:95], v[108:111], v[116:119], v[80:95]\n" \
    "buffer_load_dwordx4 v[172:175], %[cso], %[rs], 0 offen offset:3072 sc1\n" \
    "s_waitcnt vmcnt(9)\n" \
    "s_waitcnt lgkmcnt(0)\n" \
    "s_barrier\n" \
    "v_mfma_f32_32x32x16_bf16 v[64:79], v[120:123], v[124:127], v[64:79]\n" \
    "ds_read_b64_tr_b16 v[96:97], %[wa] offset:32768\n" \
    "ds_read_b64_tr_b16 v[98:99], %[wa] offset:33024\n" \
    "ds_read_b64_tr_b16 v[108:109], %[wa] offset:40960\n" \
    "ds_read_b64_tr_b16 v[110:111], %[wa] offset:41216\n" \
    "ds_read_b128 v[100:103], %[xa] offset:896\n" \
    "ds_read_b128 v[104:107], %[xa] offset:34176\n" \
    "ds_read_b128 v[112:115], %[xa] offset:928\n" \
    "ds_read_b128 v[116:119], %[xa] offset:34208\n" \
    "v_mfma_f32_32x32x16_bf16 v[80:95], v[120:123], v[128:131], v[80:95]\n" \
    "v_mfma_f32_32x32x16_bf16 v[64:79], v[132:135], v[136:139], v[64:79]\n" \
    "ds_read_b128 v[144:147], %[xe]\n" \
    "ds_read_b128 v[148:151], %[xe] offset:1024\n" \
    "ds_read_b64 v[152:153], %[xs]\n" \
    "ds_read_b64 v[154:155], %[xs] offset:256\n" \
    "ds_read_b64 %[xr0], %[xr]\n" \
    "ds_read_b64 %[xr1], %[xr] offset:16\n" \
    "v_mfma_f32_32x32x16_bf16 v[80:95], v[132:135], v[140:143], v[80:95]\n" \
    "s_waitcnt vmcnt(5)\n" \
    "s_waitcnt lgkmcnt(0)\n" \
    "s_barrier\n" \
    "v_mfma_f32_32x32x16_bf16 v[64:79], v[96:99], v[100:103], v[64:79]\n" \
    "ds_read_b64_tr_b16 v[120:121], %[wa] offset:49152\n" \
    "ds_read_b64_tr_b16 v[122:123], %[wa] offset:49408\n" \
    "ds_read_b64_tr_b16 v[132:133], %[wa] offset:57344\n" \
    "ds_read_b64_tr_b16 v[134:135], %[wa] offset:57600\n" \
    "ds_read_b128 v[124:127], %[xa] offset:960\n" \
    "ds_read_b128 v[128:131], %[xa] offset:34240\n" \
    "ds_read_b128 v[136:139], %[xa] offset:992\n" \
    "ds_read_b128 v[140:143], %[xa] offset:34272\n" \
    "v_mfma_f32_32x32x16_bf16 v[80:95], v[96:99], v[104:107], v[80:95]\n" \
    "v_mfma_f32_32x32x16_bf16 v[64:79], v[108:111], v[112:115], v[64:79]\n" \
    "v_mov_b32 v223, 0\n" \
    "s_movk_i32 s60, 4096\n" \
    "s_mov_b32 s61, 65536\n" \
    "v_mfma_f32_32x32x16_bf16 v[80:95], v[108:111], v[116:119], v[80:95]\n" \
    "s_waitcnt lgkmcnt(0)\n" \
    "v_mfma_f32_32x32x16_bf16 v[64:79], v[120:123], v[124:127], v[64:79]\n" \
    "v_mfma_f32_32x32x16_bf16 v[80:95], v[120:123], v[128:131], v[80:95]\n" \
    "v_mfma_f32_32x32x16_bf16 v[64:79], v[132:135], v[136:139], v[64:79]\n" \
    "s_waitcnt vmcnt(0)\n" \
    "v_add_f32 v160, v160, v168\n" \
    "v_add_f32 v162, v162, v170\n" \
    "v_add_f32 v164, v164, v172\n" \
    "v_add_f32 v166, v166, v174\n" \
    "v_xor_b32 v161, %[tag], v161\n" \
    "v_xor_b32 v163, %[tag], v163\n" \
    "v_xor_b32 v165, %[tag], v165\n" \
    "v_xor_b32 v167, %[tag], v167\n" \
    "v_xor_b32 v169, %[tag], v169\n" \
    "v_xor_b32 v171, %[tag], v171\n" \
    "v_xor_b32 v173, %[tag], v173\n" \
    "v_xor_b32 v175, %[tag], v175\n" \
    "v_xor_b32 v192, %[tag], v192\n" \
    "v_xor_b32 v193, %[tag], v193\n" \
    "v_xor_b32 v194, %[tag], v194\n" \
    "v_xor_b32 v195, %[tag], v195\n" \
    "v_mfma_f32_32x32x16_bf16 v[80:95], v[132:135], v[140:143], v[80:95]\n" \
    "v_add_f32 v160, v160, v164\n" \
    "v_add_f32 v162, v162, v166\n" \
    "v_or3_b32 v161, v161, v163, v165\n" \
    "v_or3_b32 v167, v167, v169, v171\n" \
    "v_or3_b32 v192, v192, v193, v194\n" \
    "v_add_f32 v156, v160, v162\n" \
    "v_add_f32 v157, v160, v162\n" \
    "v_or3_b32 v161, v161, v173, v175\n" \
    "v_or_b32 v192, v192, v195\n" \
    "v_permlane32_swap_b32 v156, v157\n" \
    "v_or_b32 v161, v161, v167\n" \
    "v_cmp_ne_u32 %[kvbad], 0, v192\n" \
    "v_add_f32 v156, v156, v157\n" \
    "v_cmp_ne_u32 %[csbad], 0, v161\n" \
    "v_cvt_pk_bf16_f32 v157, v156, v156\n" \
    "v_and_b32 v157, 0xffff0000, v157\n" \
    "v_sub_f32 v156, v156, v157\n" \
    "v_cvt_pk_bf16_f32 v158, v156, v156\n" \
    "v_and_b32 v158, 0xffff0000, v158\n" \
    "v_sub_f32 v156, v156, v158\n" \
    "v_cvt_pk_bf16_f32 v220, v157, v157\n" \
    "v_cvt_pk_bf16_f32 v221, v158, v157\n" \
    "v_cvt_pk_bf16_f32 v222, v158, v156\n" \
    "s_mov_b32 exec_lo, 0\n" \
    "v_mov_b32 v220, %[bb]\n" \
    "v_and_b32 v221, 0xffff, %[bb]\n" \
    "v_mov_b32 v222, 0\n" \
    "s_mov_b32 exec_lo, -1\n" \
    "s_cmp_lg_u64 %[csbad], 0\n" \
    "s_cbranch_scc1 10f\n" \
    "v_mfma_f32_32x32x16_bf16 v[64:79], v[220:223], v[144:147], v[64:79]\n" \
    "v_mfma_f32_32x32x16_bf16 v[80:95], v[220:223], v[148:151], v[80:95]\n" \
    "10:\n" \
    "s_cmp_lg_u64 %[kvbad], 0\n" \
    "s_cbranch_scc1 9f\n" \
    "buffer_load_dwordx4 v[196:199], %[kvg], %[rs], 0 offen sc1\n" \
    "buffer_load_dwordx4 v[200:203], %[kvg], %[rs], s61 offen sc1\n" \
    "s_add_u32 s61, s61, s60\n" \
    "buffer_load_dwordx4 v[204:207], %[kvg], %[rs], s60 offen sc1\n" \
    "buffer_load_dwordx4 v[208:211], %[kvg], %[rs], s61 offen sc1\n" \
    "s_add_u32 s61, s61, s60\n" \
    "s_add_u32 s60, s60, s60\n" \
    "buffer_load_dwordx4 v[212:215], %[kvg], %[rs], s60 offen sc1\n" \
    "buffer_load_dwordx4 v[216:219], %[kvg], %[rs], s61 offen sc1\n" \
    "9:\n"
#define P1I_ASM \
    "s_mov_b32 s61, 3\n" \
    "20:\n" \
    "s_waitcnt vmcnt(8)\n" \
    "s_waitcnt lgkmcnt(0)\n" \
    "s_barrier\n" \
    "s_add_u32 m0, %[ring], 0\n" \
    "s_nop 0\n" \
    "global_load_lds_dwordx4 %[voA], %[srcA]\n" \
    "v_add_u32 %[voA], %[stepA], %[voA]\n" \
    "v_subrev_u32 v159, %[totA], %[voA]\n" \
    "v_min_u32 %[voA], v159, %[voA]\n" \
    "s_add_u32 m0, %[ring], 1024\n" \
    "s_nop 0\n" \
    "global_load_lds_dwordx4 %[voB], %[srcB]\n" \
    "s_add_u32 m0, %[ring], 1920\n" \
    "s_nop 0\n" \
    "global_load_lds_dwordx4 %[voB], %[srcB] offset:128\n" \
    "v_add_u32 %[voB], %[stepB], %[voB]\n" \
    "v_subrev_u32 v159, %[totB], %[voB]\n" \
    "v_min_u32 %[voB], v159, %[voB]\n" \
    "s_add_u32 m0, %[ring], 3072\n" \
    "s_nop 0\n" \
    "global_load_lds_dwordx4 %[voD], %[srcB]\n" \
    "v_add_u32 %[voD], %[stepB], %[voD]\n" \
    "v_subrev_u32 v159, %[totB], %[voD]\n" \
    "v_min_u32 %[voD], v159, %[voD]\n" \
    "s_waitcnt vmcnt(8)\n" \
    "s_waitcnt lgkmcnt(0)\n" \
    "s_barrier\n" \
    "s_add_u32 m0, %[ring], 16384\n" \
    "s_nop 0\n" \
    "global_load_lds_dwordx4 %[voA], %[srcA]\n" \
    "v_add_u32 %[voA], %[stepA], %[voA]\n" \
    "v_subrev_u32 v159, %[totA], %[voA]\n" \
    "v_min_u32 %[voA], v159, %[voA]\n" \
    "s_add_u32 m0, %[ring], 17408\n" \
    "s_nop 0\n" \
    "global_load_lds_dwordx4 %[voB], %[srcB]\n" \
    "s_add_u32 m0, %[ring], 18304\n" \
    "s_nop 0\n" \
    "global_load_lds_dwordx4 %[voB], %[srcB] offset:128\n" \
    "v_add_u32 %[voB], %[stepB], %[voB]\n" \
    "v_subrev_u32 v159, %[totB], %[voB]\n" \
    "v_min_u32 %[voB], v159, %[voB]\n" \
    "s_add_u32 m0, %[ring], 19456\n" \
    "s_nop 0\n" \
    "global_load_lds_dwordx4 %[voD], %[srcB]\n" \
    "v_add_u32 %[voD], %[stepB], %[voD]\n" \
    "v_subrev_u32 v159, %[totB], %[voD]\n" \
    "v_min_u32 %[voD], v159, %[voD]\n" \
    "s_waitcnt vmcnt(8)\n" \
    "s_waitcnt lgkmcnt(0)\n" \
    "s_barrier\n" \
    "s_add_u32 m0, %[ring], 32768\n" \
    "s_nop 0\n" \
    "global_load_lds_dwordx4 %[voA], %[srcA]\n" \
    "v_add_u32 %[voA], %[stepA], %[voA]\n" \
    "v_subrev_u32 v159, %[totA], %[voA]\n" \
    "v_min_u32 %[voA], v159, %[voA]\n" \
    "s_add_u32 m0, %[ring], 33792\n" \
    "s_nop 0\n" \
    "global_load_lds_dwordx4 %[voB], %[srcB]\n" \
    "s_add_u32 m0, %[ring], 34688\n" \
    "s_nop 0\n" \
    "global_load_lds_dwordx4 %[voB], %[srcB] offset:128\n" \
    "v_add_u32 %[voB], %[stepB], %[voB]\n" \
    "v_subrev_u32 v159, %[totB], %[voB]\n" \
    "v_min_u32 %[voB], v159, %[voB]\n" \
    "s_add_u32 m0, %[ring], 35840\n" \
    "s_nop 0\n" \
    "global_load_lds_dwordx4 %[voD], %[srcB]\n" \
    "v_add_u32 %[voD], %[stepB], %[voD]\n" \
    "v_subrev_u32 v159, %[totB], %[voD]\n" \
    "v_min_u32 %[voD], v159, %[voD]\n" \
    "s_waitcnt vmcnt(8)\n" \
    "s_waitcnt lgkmcnt(0)\n" \
    "s_barrier\n" \
    "s_add_u32 m0, %[ring], 49152\n" \
    "s_nop 0\n" \
    "global_load_lds_dwordx4 %[voA], %[srcA]\n" \
    "v_add_u32 %[voA], %[stepA], %[voA]\n" \
    "v_subrev_u32 v159, %[totA], %[voA]\n" \
    "v_min_u32 %[voA], v159, %[voA]\n" \
    "s_add_u32 m0, %[ring], 50176\n" \
    "s_nop 0\n" \
    "global_load_lds_dwordx4 %[voB], %[srcB]\n" \
    "s_add_u32 m0, %[ring], 51072\n" \
    "s_nop 0\n" \
    "global_load_lds_dwordx4 %[voB], %[srcB] offset:128\n" \
    "v_add_u32 %[voB], %[stepB], %[voB]\n" \
    "v_subrev_u32 v159, %[totB], %[voB]\n" \
    "v_min_u32 %[voB], v159, %[voB]\n" \
    "s_add_u32 m0, %[ring], 52224\n" \
    "s_nop 0\n" \
    "global_load_lds_dwordx4 %[voD], %[srcB]\n" \
    "v_add_u32 %[voD], %[stepB], %[voD]\n" \
    "v_subrev_u32 v159, %[totB], %[voD]\n" \
    "v_min_u32 %[voD], v159, %[voD]\n" \
    "s_sub_u32 s61, s61, 1\n" \
    "s_cmp_lg_u32 s61, 0\n" \
    "s_cbranch_scc1 20b\n" \
    "s_waitcnt vmcnt(8)\n" \
    "s_waitcnt lgkmcnt(0)\n" \
    "s_barrier\n" \
    "buffer_load_dwordx4 v[192:195], %[flo], %[rs], 0 offen sc1\n" \
    "s_waitcnt vmcnt(5)\n" \
    "s_waitcnt lgkmcnt(0)\n" \
    "s_barrier\n" \
    "ds_read_b64 %[xr0], %[xr]\n" \
    "ds_read_b64 %[xr1], %[xr] offset:16\n" \
    "s_waitcnt vmcnt(1)\n" \
    "s_waitcnt lgkmcnt(0)\n" \
    "s_barrier\n" \
    "v_mov_b32 v223, 0\n" \
    "s_movk_i32 s60, 4096\n" \
    "s_mov_b32 s61, 65536\n" \
    "s_waitcnt lgkmcnt(0)\n" \
    "s_waitcnt vmcnt(0)\n" \
    "v_xor_b32 v192, %[tag], v192\n" \
    "v_xor_b32 v193, %[tag], v193\n" \
    "v_xor_b32 v194, %[tag], v194\n" \
    "v_xor_b32 v195, %[tag], v195\n" \
    "v_or3_b32 v192, v192, v193, v194\n" \
    "v_or_b32 v192, v192, v195\n" \
    "v_cmp_ne_u32 %[kvbad], 0, v192\n" \
    "s_nop 3\n" \
    "s_cmp_lg_u64 %[kvbad], 0\n" \
    "s_cbranch_scc1 9f\n" \
    "buffer_load_dwordx4 v[196:199], %[kvg], %[rs], 0 offen sc1\n" \
    "buffer_load_dwordx4 v[200:203], %[kvg], %[rs], s61 offen sc1\n" \
    "s_add_u32 s61, s61, s60\n" \
    "buffer_load_dwordx4 v[204:207], %[kvg], %[rs], s60 offen sc1\n" \
    "buffer_load_dwordx4 v[208:211], %[kvg], %[rs], s61 offen sc1\n" \
    "s_add_u32 s61, s61, s60\n" \
    "s_add_u32 s60, s60, s60\n" \
    "buffer_load_dwordx4 v[212:215], %[kvg], %[rs], s60 offen sc1\n" \
    "buffer_load_dwordx4 v[216:219], %[kvg], %[rs], s61 offen sc1\n" \
    "9:\n"
#define P1_CLOB "v96", "v97", "v98", "v99", "v100", "v101", "v102", "v103", "v104", "v105", "v106", "v107", "v108", "v109", "v110", "v111", "v112", "v113", "v114", "v115", "v116", "v117", "v118", "v119", "v120", "v121", "v122", "v123", "v124", "v125", "v126", "v127", "v128", "v129", "v130", "v131", "v132", "v133", "v134", "v135", "v136", "v137", "v138", "v139", "v140", "v141", "v142", "v143", "v156", "v157", "v158", "v159", "v160", "v161", "v162", "v163", "v164", "v165", "v166", "v167", "v168", "v169", "v170", "v171", "v172", "v173", "v174", "v175", "v176", "v177", "v178", "v179", "v180", "v181", "v182", "v183", "v184", "v185", "v186", "v187", "v188", "v189", "v190", "v191", "v192", "v193", "v194", "v195", "v220", "v221", "v222", "v223"
#define QEPI_ASM \
    "v_mul_f32 v152, 0x3eb8aa3b, v152\n" \
    "v_mul_f32 v154, 0x3eb8aa3b, v154\n" \
    "s_nop 12\n" \
    "v_pk_mul_f32 v[64:65], v[64:65], v[152:153] op_sel_hi:[1,0]\n" \
    "v_pk_mul_f32 v[66:67], v[66:67], v[152:153] op_sel_hi:[1,0]\n" \
    "v_pk_mul_f32 v[68:69], v[68:69], v[152:153] op_sel_hi:[1,0]\n" \
    "v_pk_mul_f32 v[70:71], v[70:71], v[152:153] op_sel_hi:[1,0]\n" \
    "v_cvt_pk_bf16_f32 v96, v64, v65\n" \
    "v_cvt_pk_bf16_f32 v97, v66, v67\n" \
    "v_cvt_pk_bf16_f32 v98, v68, v69\n" \
    "v_cvt_pk_bf16_f32 v99, v70, v71\n" \
    "ds_write_b128 %[qw], v[96:99] offset:0\n" \
    "v_pk_mul_f32 v[72:73], v[72:73], v[152:153] op_sel_hi:[1,0]\n" \
    "v_pk_mul_f32 v[74:75], v[74:75], v[152:153] op_sel_hi:[1,0]\n" \
    "v_pk_mul_f32 v[76:77], v[76:77], v[152:153] op_sel_hi:[1,0]\n" \
    "v_pk_mul_f32 v[78:79], v[78:79], v[152:153] op_sel_hi:[1,0]\n" \
    "v_cvt_pk_bf16_f32 v100, v72, v73\n" \
    "v_cvt_pk_bf16_f32 v101, v74, v75\n" \
    "v_cvt_pk_bf16_f32 v102, v76, v77\n" \
    "v_cvt_pk_bf16_f32 v103, v78, v79\n" \
    "ds_write_b128 %[qw], v[100:103] offset:1024\n" \
    "v_pk_mul_f32 v[80:81], v[80:81], v[154:155] op_sel_hi:[1,0]\n" \
    "v_pk_mul_f32 v[82:83], v[82:83], v[154:155] op_sel_hi:[1,0]\n" \
    "v_pk_mul_f32 v[84:85], v[84:85], v[154:155] op_sel_hi:[1,0]\n" \
    "v_pk_mul_f32 v[86:87], v[86:87], v[154:155] op_sel_hi:[1,0]\n" \
    "v_cvt_pk_bf16_f32 v104, v80, v81\n" \
    "v_cvt_pk_bf16_f32 v105, v82, v83\n" \
    "v_cvt_pk_bf16_f32 v106, v84, v85\n" \
    "v_cvt_pk_bf16_f32 v107, v86, v87\n" \
    "ds_write_b128 %[qw], v[104:107] offset:2048\n" \
    "v_pk_mul_f32 v[88:89], v[88:89], v[154:155] op_sel_hi:[1,0]\n" \
    "v_pk_mul_f32 v[90:91], v[90:91], v[154:155] op_sel_hi:[1,0]\n" \
    "v_pk_mul_f32 v[92:93], v[92:93], v[154:155] op_sel_hi:[1,0]\n" \
    "v_pk_mul_f32 v[94:95], v[94:95], v[154:155] op_sel_hi:[1,0]\n" \
    "v_cvt_pk_bf16_f32 v108, v88, v89\n" \
    "v_cvt_pk_bf16_f32 v109, v90, v91\n" \
    "v_cvt_pk_bf16_f32 v110, v92, v93\n" \
    "v_cvt_pk_bf16_f32 v111, v94, v95\n" \
    "ds_write_b128 %[qw], v[108:111] offset:3072\n"
#define GELU_ASM \
    "v_mul_f32 v156, 0xc0135761, v152\n" \
    "v_mul_f32 v157, v152, v152\n" \
    "v_mul_f32 v157, v157, v152\n" \
    "v_mul_f32 v157, 0xbdd2d3e8, v157\n" \
    "v_mul_f32 v158, 0xc0135761, v154\n" \
    "v_mul_f32 v159, v154, v154\n" \
    "v_mul_f32 v159, v159, v154\n" \
    "v_mul_f32 v159, 0xbdd2d3e8, v159\n" \
    "s_nop 7\n" \
    "v_pk_mul_f32 v[96:97], v[64:65], v[64:65]\n" \
    "v_pk_mul_f32 v[98:99], v[66:67], v[66:67]\n" \
    "v_pk_mul_f32 v[100:101], v[68:69], v[68:69]\n" \
    "v_pk_mul_f32 v[102:103], v[70:71], v[70:71]\n" \
    "v_pk_fma_f32 v[96:97], v[96:97], v[156:157], v[156:157] op_sel:[0,1,0] op_sel_hi:[1,1,0]\n" \
    "v_pk_fma_f32 v[98:99], v[98:99], v[156:157], v[156:157] op_sel:[0,1,0] op_sel_hi:[1,1,0]\n" \
    "v_pk_fma_f32 v[100:101], v[100:101], v[156:157], v[156:157] op_sel:[0,1,0] op_sel_hi:[1,1,0]\n" \
    "v_pk_fma_f32 v[102:103], v[102:103], v[156:157], v[156:157] op_sel:[0,1,0] op_sel_hi:[1,1,0]\n" \
    "v_pk_mul_f32 v[96:97], v[64:65], v[96:97]\n" \
    "v_pk_mul_f32 v[98:99], v[66:67], v[98:99]\n" \
    "v_pk_mul_f32 v[100:101], v[68:69], v[100:101]\n" \
    "v_pk_mul_f32 v[102:103], v[70:71], v[102:103]\n" \
    "v_exp_f32 v96, v96\n" \
    "v_exp_f32 v97, v97\n" \
    "v_exp_f32 v98, v98\n" \
    "v_exp_f32 v99, v99\n" \
    "v_exp_f32 v100, v100\n" \
    "v_exp_f32 v101, v101\n" \
    "v_exp_f32 v102, v102\n" \
    "v_exp_f32 v103, v103\n" \
    "v_pk_fma_f32 v[96:97], v[96:97], v[152:153], v[152:153] op_sel:[0,1,1] op_sel_hi:[1,1,1]\n" \
    "v_pk_fma_f32 v[98:99], v[98:99], v[152:153], v[152:153] op_sel:[0,1,1] op_sel_hi:[1,1,1]\n" \
    "v_pk_fma_f32 v[100:101], v[100:101], v[152:153], v[152:153] op_sel:[0,1,1] op_sel_hi:[1,1,1]\n" \
    "v_pk_fma_f32 v[102:103], v[102:103], v[152:153], v[152:153] op_sel:[0,1,1] op_sel_hi:[1,1,1]\n" \
    "v_rcp_f32 v96, v96\n" \
    "v_rcp_f32 v97, v97\n" \
    "v_rcp_f32 v98, v98\n" \
    "v_rcp_f32 v99, v99\n" \
    "v_rcp_f32 v100, v100\n" \
    "v_rcp_f32 v101, v101\n" \
    "v_rcp_f32 v102, v102\n" \
    "v_rcp_f32 v103, v103\n" \
    "v_pk_mul_f32 v[96:97], v[64:65], v[96:97]\n" \
    "v_pk_mul_f32 v[98:99], v[66:67], v[98:99]\n" \
    "v_pk_mul_f32 v[100:101], v[68:69], v[100:101]\n" \
    "v_pk_mul_f32 v[102:103], v[70:71], v[102:103]\n" \
    "v_cvt_pk_bf16_f32 v160, v96, v97\n" \
    "v_cvt_pk_bf16_f32 v161, v98, v99\n" \
    "v_cvt_pk_bf16_f32 v162, v100, v101\n" \
    "v_cvt_pk_bf16_f32 v163, v102, v103\n" \
    "v_pk_mul_f32 v[112:113], v[72:73], v[72:73]\n" \
    "v_pk_mul_f32 v[114:115], v[74:75], v[74:75]\n" \
    "v_pk_mul_f32 v[116:117], v[76:77], v[76:77]\n" \
    "v_pk_mul_f32 v[118:119], v[78:79], v[78:79]\n" \
    "v_pk_fma_f32 v[112:113], v[112:113], v[156:157], v[156:157] op_sel:[0,1,0] op_sel_hi:[1,1,0]\n" \
    "v_pk_fma_f32 v[114:115], v[114:115], v[156:157], v[156:157] op_sel:[0,1,0] op_sel_hi:[1,1,0]\n" \
    "v_pk_fma_f32 v[116:117], v[116:117], v[156:157], v[156:157] op_sel:[0,1,0] op_sel_hi:[1,1,0]\n" \
    "v_pk_fma_f32 v[118:119], v[118:119], v[156:157], v[156:157] op_sel:[0,1,0] op_sel_hi:[1,1,0]\n" \
    "v_pk_mul_f32 v[112:113], v[72:73], v[112:113]\n" \
    "v_pk_mul_f32 v[114:115], v[74:75], v[114:115]\n" \
    "v_pk_mul_f32 v[116:117], v[76:77], v[116:117]\n" \
    "v_pk_mul_f32 v[118:119], v[78:79], v[118:119]\n" \
    "v_exp_f32 v112, v112\n" \
    "v_exp_f32 v113, v113\n" \
    "v_exp_f32 v114, v114\n" \
    "v_exp_f32 v115, v115\n" \
    "v_exp_f32 v116, v116\n" \
    "v_exp_f32 v117, v117\n" \
    "v_exp_f32 v118, v118\n" \
    "v_exp_f32 v119, v119\n" \
    "v_pk_fma_f32 v[112:113], v[112:113], v[152:153], v[152:153] op_sel:[0,1,1] op_sel_hi:[1,1,1]\n" \
    "v_pk_fma_f32 v[114:115], v[114:115], v[152:153], v[152:153] op_sel:[0,1,1] op_sel_hi:[1,1,1]\n" \
    "v_pk_fma_f32 v[116:117], v[116:117], v[152:153], v[152:153] op_sel:[0,1,1] op_sel_hi:[1,1,1]\n" \
    "v_pk_fma_f32 v[118:119], v[118:119], v[152:153], v[152:153] op_sel:[0,1,1] op_sel_hi:[1,1,1]\n" \
    "v_rcp_f32 v112, v112\n" \
    "v_rcp_f32 v113, v113\n" \
    "v_rcp_f32 v114, v114\n" \
    "v_rcp_f32 v115, v115\n" \
    "v_rcp_f32 v116, v116\n" \
    "v_rcp_f32 v117, v117\n" \
    "v_rcp_f32 v118, v118\n" \
    "v_rcp_f32 v119, v119\n" \
    "v_pk_mul_f32 v[112:113], v[72:73], v[112:113]\n" \
    "v_pk_mul_f32 v[114:115], v[74:75], v[114:115]\n" \
    "v_pk_mul_f32 v[116:117], v[76:77], v[116:117]\n" \
    "v_pk_mul_f32 v[118:119], v[78:79], v[118:119]\n" \
    "v_cvt_pk_bf16_f32 v164, v112, v113\n" \
    "v_cvt_pk_bf16_f32 v165, v114, v115\n" \
    "v_cvt_pk_bf16_f32 v166, v116, v117\n" \
    "v_cvt_pk_bf16_f32 v167, v118, v119\n" \
    "v_pk_mul_f32 v[96:97], v[80:81], v[80:81]\n" \
    "v_pk_mul_f32 v[98:99], v[82:83], v[82:83]\n" \
    "v_pk_mul_f32 v[100:101], v[84:85], v[84:85]\n" \
    "v_pk_mul_f32 v[102:103], v[86:87], v[86:87]\n" \
    "v_pk_fma_f32 v[96:97], v[96:97], v[158:159], v[158:159] op_sel:[0,1,0] op_sel_hi:[1,1,0]\n" \
    "v_pk_fma_f32 v[98:99], v[98:99], v[158:159], v[158:159] op_sel:[0,1,0] op_sel_hi:[1,1,0]\n" \
    "v_pk_fma_f32 v[100:101], v[100:101], v[158:159], v[158:159] op_sel:[0,1,0] op_sel_hi:[1,1,0]\n" \
    "v_pk_fma_f32 v[102:103], v[102:103], v[158:159], v[158:159] op_sel:[0,1,0] op_sel_hi:[1,1,0]\n" \
    "v_pk_mul_f32 v[96:97], v[80:81], v[96:97]\n" \
    "v_pk_mul_f32 v[98:99], v[82:83], v[98:99]\n" \
    "v_pk_mul_f32 v[100:101], v[84:85], v[100:101]\n" \
    "v_pk_mul_f32 v[102:103], v[86:87], v[102:103]\n" \
    "v_exp_f32 v96, v96\n" \
    "v_exp_f32 v97, v97\n" \
    "v_exp_f32 v98, v98\n" \
    "v_exp_f32 v99, v99\n" \
    "v_exp_f32 v100, v100\n" \
    "v_exp_f32 v101, v101\n" \
    "v_exp_f32 v102, v102\n" \
    "v_exp_f32 v103, v103\n" \
    "v_pk_fma_f32 v[96:97], v[96:97], v[154:155], v[154:155] op_sel:[0,1,1] op_sel_hi:[1,1,1]\n" \
    "v_pk_fma_f32 v[98:99], v[98:99], v[154:155], v[154:155] op_sel:[0,1,1] op_sel_hi:[1,1,1]\n" \
    "v_pk_fma_f32 v[100:101], v[100:101], v[154:155], v[154:155] op_sel:[0,1,1] op_sel_hi:[1,1,1]\n" \
    "v_pk_fma_f32 v[102:103], v[102:103], v[154:155], v[154:155] op_sel:[0,1,1] op_sel_hi:[1,1,1]\n" \
    "v_rcp_f32 v96, v96\n" \
    "v_rcp_f32 v97, v97\n" \
    "v_rcp_f32 v98, v98\n" \
    "v_rcp_f32 v99, v99\n" \
    "v_rcp_f32 v100, v100\n" \
    "v_rcp_f32 v101, v101\n" \
    "v_rcp_f32 v102, v102\n" \
    "v_rcp_f32 v103, v103\n" \
    "v_pk_mul_f32 v[96:97], v[80:81], v[96:97]\n" \
    "v_pk_mul_f32 v[98:99], v[82:83], v[98:99]\n" \
    "v_pk_mul_f32 v[100:101], v[84:85], v[100:101]\n" \
    "v_pk_mul_f32 v[102:103], v[86:87], v[102:103]\n" \
    "v_cvt_pk_bf16_f32 v168, v96, v97\n" \
    "v_cvt_pk_bf16_f32 v169, v98, v99\n" \
    "v_cvt_pk_bf16_f32 v170, v100, v101\n" \
    "v_cvt_pk_bf16_f32 v171, v102, v103\n" \
    "v_pk_mul_f32 v[112:113], v[88:89], v[88:89]\n" \
    "v_pk_mul_f32 v[114:115], v[90:91], v[90:91]\n" \
    "v_pk_mul_f32 v[116:117], v[92:93], v[92:93]\n" \
    "v_pk_mul_f32 v[118:119], v[94:95], v[94:95]\n" \
    "v_pk_fma_f32 v[112:113], v[112:113], v[158:159], v[158:159] op_sel:[0,1,0] op_sel_hi:[1,1,0]\n" \
    "v_pk_fma_f32 v[114:115], v[114:115], v[158:159], v[158:159] op_sel:[0,1,0] op_sel_hi:[1,1,0]\n" \
    "v_pk_fma_f32 v[116:117], v[116:117], v[158:159], v[158:159] op_sel:[0,1,0] op_sel_hi:[1,1,0]\n" \
    "v_pk_fma_f32 v[118:119], v[118:119], v[158:159], v[158:159] op_sel:[0,1,0] op_sel_hi:[1,1,0]\n" \
    "v_pk_mul_f32 v[112:113], v[88:89], v[112:113]\n" \
    "v_pk_mul_f32 v[114:115], v[90:91], v[114:115]\n" \
    "v_pk_mul_f32 v[116:117], v[92:93], v[116:117]\n" \
    "v_pk_mul_f32 v[118:119], v[94:95], v[118:119]\n" \
    "v_exp_f32 v112, v112\n" \
    "v_exp_f32 v113, v113\n" \
    "v_exp_f32 v114, v114\n" \
    "v_exp_f32 v115, v115\n" \
    "v_exp_f32 v116, v116\n" \
    "v_exp_f32 v117, v117\n" \
    "v_exp_f32 v118, v118\n" \
    "v_exp_f32 v119, v119\n" \
    "v_pk_fma_f32 v[112:113], v[112:113], v[154:155], v[154:155] op_sel:[0,1,1] op_sel_hi:[1,1,1]\n" \
    "v_pk_fma_f32 v[114:115], v[114:115], v[154:155], v[154:155] op_sel:[0,1,1] op_sel_hi:[1,1,1]\n" \
    "v_pk_fma_f32 v[116:117], v[116:117], v[154:155], v[154:155] op_sel:[0,1,1] op_sel_hi:[1,1,1]\n" \
    "v_pk_fma_f32 v[118:119], v[118:119], v[154:155], v[154:155] op_sel:[0,1,1] op_sel_hi:[1,1,1]\n" \
    "v_rcp_f32 v112, v112\n" \
    "v_rcp_f32 v113, v113\n" \
    "v_rcp_f32 v114, v114\n" \
    "v_rcp_f32 v115, v115\n" \
    "v_rcp_f32 v116, v116\n" \
    "v_rcp_f32 v117, v117\n" \
    "v_rcp_f32 v118, v118\n" \
    "v_rcp_f32 v119, v119\n" \
    "v_pk_mul_f32 v[112:113], v[88:89], v[112:113]\n" \
    "v_pk_mul_f32 v[114:115], v[90:91], v[114:115]\n" \
    "v_pk_mul_f32 v[116:117], v[92:93], v[116:117]\n" \
    "v_pk_mul_f32 v[118:119], v[94:95], v[118:119]\n" \
    "v_cvt_pk_bf16_f32 v172, v112, v113\n" \
    "v_cvt_pk_bf16_f32 v173, v114, v115\n" \
    "v_cvt_pk_bf16_f32 v174, v116, v117\n" \
    "v_cvt_pk_bf16_f32 v175, v118, v119\n"
#define EPI_CLOB "v96", "v97", "v98", "v99", "v100", "v101", "v102", "v103", "v104", "v105", "v106", "v107", "v108", "v109", "v110", "v111", "v112", "v113", "v114", "v115", "v116", "v117", "v118", "v119", "v120", "v121", "v122", "v123", "v124", "v125", "v126", "v127", "v128", "v129", "v130", "v131", "v132", "v133", "v134", "v135", "v156", "v157", "v158", "v159"
#define ATT_ASM \
    "v_mov_b32 v47, 0xdddddddd\n" \
    "v_mov_b32 v112, 0x3f803f80\n" \
    "s_sub_u32 s60, %[w], 2\n" \
    "s_cmp_lt_u32 s60, %[nft]\n" \
    "v_cndmask_b32 v112, v112, 0, %[m1]\n" \
    "s_mov_b32 s60, 88064\n" \
    "s_waitcnt vmcnt(0)\n" \
    "ds_write_b128 %[kw], v[196:199]\n" \
    "ds_write_b128 %[vw], v[200:203]\n" \
    "v_mov_b32 v113, v112\n" \
    "v_mov_b32 v114, v112\n" \
    "v_mov_b32 v115, v112\n" \
    "s_mov_b64 exec, 15\n" \
    "ds_write_b128 %[cw], v[112:115]\n" \
    "s_mov_b64 exec, -1\n" \
    "s_cbranch_scc0 1f\n" \
    "buffer_store_dwordx4 v[160:163], %[hso], %[rs], 0 offen offset:0 sc1\n" \
    "buffer_store_dwordx4 v[164:167], %[hso], %[rs], 0 offen offset:1024 sc1\n" \
    "buffer_store_dwordx4 v[168:171], %[hso], %[rs], s60 offen offset:0 sc1\n" \
    "buffer_store_dwordx4 v[172:175], %[hso], %[rs], s60 offen offset:1024 sc1\n" \
    "1:\n" \
    "s_waitcnt lgkmcnt(0)\n" \
    "s_barrier\n" \
    "ds_read_b128 v[128:131], %[qa]\n" \
    "ds_read_b128 v[136:139], %[ka]\n" \
    "ds_read_b128 v[132:135], %[qa] offset:1024\n" \
    "ds_read_b128 v[144:147], %[va]\n" \
    "ds_read_b128 v[148:151], %[va] offset:544\n" \
    "ds_write_b128 %[kw], v[204:207] offset:16896\n" \
    "ds_write_b128 %[vw], v[208:211] offset:16896\n" \
    "s_mov_b64 exec, 15\n" \
    "ds_write_b128 %[cw], v[112:115] offset:16896\n" \
    "ds_write_b128 %[cw], v[112:115] offset:33792\n" \
    "s_mov_b64 exec, -1\n" \
    "s_waitcnt lgkmcnt(7)\n" \
    "v_mfma_f32_32x32x16_bf16 v[96:111], v[136:139], v[128:131], 0\n" \
    "s_movk_i32 s60, 12288\n" \
    "s_mov_b32 s61, 77824\n" \
    "buffer_load_dwordx4 v[176:179], %[kvg], %[rs], s60 offen sc1\n" \
    "buffer_load_dwordx4 v[180:183], %[kvg], %[rs], s61 offen sc1\n" \
    "v_mov_b32 v64, 0\n" \
    "v_mov_b32 v80, 0\n" \
    "v_mov_b32 v65, 0\n" \
    "v_mov_b32 v81, 0\n" \
    "v_mov_b32 v66, 0\n" \
    "v_mov_b32 v82, 0\n" \
    "v_mov_b32 v67, 0\n" \
    "v_mov_b32 v83, 0\n" \
    "v_mov_b32 v68, 0\n" \
    "v_mov_b32 v84, 0\n" \
    "v_mov_b32 v69, 0\n" \
    "v_mov_b32 v85, 0\n" \
    "v_mov_b32 v70, 0\n" \
    "v_mov_b32 v86, 0\n" \
    "v_mov_b32 v71, 0\n" \
    "v_mov_b32 v87, 0\n" \
    "v_mov_b32 v72, 0\n" \
    "v_mov_b32 v88, 0\n" \
    "v_mov_b32 v73, 0\n" \
    "v_mov_b32 v89, 0\n" \
    "v_mov_b32 v74, 0\n" \
    "v_mov_b32 v90, 0\n" \
    "v_mov_b32 v75, 0\n" \
    "v_mov_b32 v91, 0\n" \
    "v_mov_b32 v76, 0\n" \
    "v_mov_b32 v92, 0\n" \
    "v_mov_b32 v77, 0\n" \
    "v_mov_b32 v93, 0\n" \
    "v_mov_b32 v78, 0\n" \
    "v_mov_b32 v94, 0\n" \
    "v_mov_b32 v79, 0\n" \
    "v_mov_b32 v95, 0\n" \
    "v_mov_b32 v120, 0\n" \
    "v_mov_b32 v121, 0\n" \
    "v_mov_b32 v122, 0\n" \
    "v_mov_b32 v123, 0\n" \
    "v_mov_b32 v124, 0\n" \
    "v_mov_b32 v125, 0\n" \
    "v_mov_b32 v126, 0\n" \
    "v_mov_b32 v127, 0\n" \
    "v_mov_b32 v156, 0\n" \
    "v_mov_b32 v157, 0\n" \
    "v_mov_b32 v158, 0\n" \
    "v_mov_b32 v159, 0\n" \
    "s_waitcnt lgkmcnt(4)\n" \
    "s_setprio 3\n" \
    "v_exp_f32 v96, v96\n" \
    "v_exp_f32 v97, v97\n" \
    "v_smfmac_f32_32x32x32_bf16 v[80:95], v[156:159], v[120:127], v47\n" \
    "ds_read_b128 v[140:143], %[ka] offset:1024\n" \
    "ds_read_b128 v[152:155], %[va] offset:1088\n" \
    "ds_read_b128 v[156:159], %[va] offset:1632\n" \
    "v_exp_f32 v98, v98\n" \
    "v_exp_f32 v99, v99\n" \
    "v_exp_f32 v100, v100\n" \
    "v_mfma_f32_32x32x16_bf16 v[112:127], v[136:139], v[132:135], 0\n" \
    "s_movk_i32 s60, 16384\n" \
    "buffer_load_dwordx4 v[184:187], %[kvg], %[rs], s60 offen sc1\n" \
    "v_exp_f32 v101, v101\n" \
    "v_exp_f32 v102, v102\n" \
    "v_exp_f32 v103, v103\n" \
    "v_exp_f32 v104, v104\n" \
    "v_exp_f32 v105, v105\n" \
    "v_smfmac_f32_32x32x32_bf16 v[64:79], v[144:147], v[96:103], v47\n" \
    "s_mov_b32 s60, 81920\n" \
    "buffer_load_dwordx4 v[188:191], %[kvg], %[rs], s60 offen sc1\n" \
    "v_exp_f32 v106, v106\n" \
    "v_exp_f32 v107, v107\n" \
    "v_exp_f32 v108, v108\n" \
    "v_exp_f32 v109, v109\n" \
    "v_exp_f32 v110, v110\n" \
    "v_exp_f32 v111, v111\n" \
    "v_exp_f32 v112, v112\n" \
    "v_exp_f32 v113, v113\n" \
    "v_smfmac_f32_32x32x32_bf16 v[64:79], v[148:151], v[104:111], v47\n" \
    "s_movk_i32 s60, 20480\n" \
    "buffer_load_dwordx4 v[224:227], %[kvg], %[rs], s60 offen sc1\n" \
    "v_exp_f32 v114, v114\n" \
    "v_exp_f32 v115, v115\n" \
    "v_exp_f32 v116, v116\n" \
    "s_waitcnt lgkmcnt(0)\n" \
    "v_mfma_f32_32x32x16_bf16 v[96:111], v[140:143], v[128:131], 0\n" \
    "ds_write_b128 %[kw], v[212:215] offset:33792\n" \
    "v_exp_f32 v117, v117\n" \
    "v_exp_f32 v118, v118\n" \
    "v_exp_f32 v119, v119\n" \
    "v_exp_f32 v120, v120\n" \
    "v_exp_f32 v121, v121\n" \
    "v_smfmac_f32_32x32x32_bf16 v[80:95], v[144:147], v[112:119], v47\n" \
    "v_exp_f32 v122, v122\n" \
    "v_exp_f32 v123, v123\n" \
    "v_exp_f32 v124, v124\n" \
    "v_exp_f32 v125, v125\n" \
    "v_exp_f32 v126, v126\n" \
    "v_exp_f32 v127, v127\n" \
    "s_setprio 2\n" \
    "v_exp_f32 v96, v96\n" \
    "v_exp_f32 v97, v97\n" \
    "v_smfmac_f32_32x32x32_bf16 v[80:95], v[148:151], v[120:127], v47\n" \
    "ds_read_b128 v[136:139], %[ka] offset:2048\n" \
    "ds_read_b128 v[144:147], %[va] offset:2176\n" \
    "ds_read_b128 v[148:151], %[va] offset:2720\n" \
    "v_exp_f32 v98, v98\n" \
    "v_exp_f32 v99, v99\n" \
    "v_exp_f32 v100, v100\n" \
    "v_mfma_f32_32x32x16_bf16 v[112:127], v[140:143], v[132:135], 0\n" \
    "s_mov_b32 s60, 86016\n" \
    "buffer_load_dwordx4 v[228:231], %[kvg], %[rs], s60 offen sc1\n" \
    "v_exp_f32 v101, v101\n" \
    "v_exp_f32 v102, v102\n" \
    "v_exp_f32 v103, v103\n" \
    "v_exp_f32 v104, v104\n" \
    "v_exp_f32 v105, v105\n" \
    "v_smfmac_f32_32x32x32_bf16 v[64:79], v[152:155], v[96:103], v47\n" \
    "s_movk_i32 s60, 24576\n" \
    "buffer_load_dwordx4 v[232:235], %[kvg], %[rs], s60 offen sc1\n" \
    "v_exp_f32 v106, v106\n" \
    "v_exp_f32 v107, v107\n" \
    "v_exp_f32 v108, v108\n" \
    "v_exp_f32 v109, v109\n" \
    "v_exp_f32 v110, v110\n" \
    "v_exp_f32 v111, v111\n" \
    "v_exp_f32 v112, v112\n" \
    "v_exp_f32 v113, v113\n" \
    "v_smfmac_f32_32x32x32_bf16 v[64:79], v[156:159], v[104:111], v47\n" \
    "s_mov_b32 s60, 90112\n" \
    "buffer_load_dwordx4 v[236:239], %[kvg], %[rs], s60 offen sc1\n" \
    "v_exp_f32 v114, v114\n" \
    "v_exp_f32 v115, v115\n" \
    "v_exp_f32 v116, v116\n" \
    "s_waitcnt lgkmcnt(0)\n" \
    "v_mfma_f32_32x32x16_bf16 v[96:111], v[136:139], v[128:131], 0\n" \
    "ds_write_b128 %[vw], v[216:219] offset:33792\n" \
    "v_exp_f32 v117, v117\n" \
    "v_exp_f32 v118, v118\n" \
    "v_exp_f32 v119, v119\n" \
    "v_exp_f32 v120, v120\n" \
    "v_exp_f32 v121, v121\n" \
    "v_smfmac_f32_32x32x32_bf16 v[80:95], v[152:155], v[112:119], v47\n" \
    "v_exp_f32 v122, v122\n" \
    "v_exp_f32 v123, v123\n" \
    "v_exp_f32 v124, v124\n" \
    "v_exp_f32 v125, v125\n" \
    "v_exp_f32 v126, v126\n" \
    "v_exp_f32 v127, v127\n" \
    "s_setprio 1\n" \
    "v_exp_f32 v96, v96\n" \
    "v_exp_f32 v97, v97\n" \
    "v_smfmac_f32_32x32x32_bf16 v[80:95], v[156:159], v[120:127], v47\n" \
    "ds_read_b128 v[140:143], %[ka] offset:3072\n" \
    "ds_read_b128 v[152:155], %[va] offset:3264\n" \
    "ds_read_b128 v[156:159], %[va] offset:3808\n" \
    "v_exp_f32 v98, v98\n" \
    "v_exp_f32 v99, v99\n" \
    "v_exp_f32 v100, v100\n" \
    "v_mfma_f32_32x32x16_bf16 v[112:127], v[136:139], v[132:135], 0\n" \
    "s_movk_i32 s60, 28672\n" \
    "buffer_load_dwordx4 v[240:243], %[kvg], %[rs], s60 offen sc1\n" \
    "v_exp_f32 v101, v101\n" \
    "v_exp_f32 v102, v102\n" \
    "v_exp_f32 v103, v103\n" \
    "v_exp_f32 v104, v104\n" \
    "v_exp_f32 v105, v105\n" \
    "v_smfmac_f32_32x32x32_bf16 v[64:79], v[144:147], v[96:103], v47\n" \
    "s_mov_b32 s60, 94208\n" \
    "buffer_load_dwordx4 v[244:247], %[kvg], %[rs], s60 offen sc1\n" \
    "v_exp_f32 v106, v106\n" \
    "v_exp_f32 v107, v107\n" \
    "v_exp_f32 v108, v108\n" \
    "v_exp_f32 v109, v109\n" \
    "v_exp_f32 v110, v110\n" \
    "v_exp_f32 v111, v111\n" \
    "v_exp_f32 v112, v112\n" \
    "v_exp_f32 v113, v113\n" \
    "v_smfmac_f32_32x32x32_bf16 v[64:79], v[148:151], v[104:111], v47\n" \
    "v_exp_f32 v114, v114\n" \
    "v_exp_f32 v115, v115\n" \
    "v_exp_f32 v116, v116\n" \
    "s_waitcnt lgkmcnt(0)\n" \
    "v_mfma_f32_32x32x16_bf16 v[96:111], v[140:143], v[128:131], 0\n" \
    "v_exp_f32 v117, v117\n" \
    "v_exp_f32 v118, v118\n" \
    "v_exp_f32 v119, v119\n" \
    "v_exp_f32 v120, v120\n" \
    "v_exp_f32 v121, v121\n" \
    "v_smfmac_f32_32x32x32_bf16 v[80:95], v[144:147], v[112:119], v47\n" \
    "v_exp_f32 v122, v122\n" \
    "v_exp_f32 v123, v123\n" \
    "v_exp_f32 v124, v124\n" \
    "v_exp_f32 v125, v125\n" \
    "v_exp_f32 v126, v126\n" \
    "v_exp_f32 v127, v127\n" \
    "s_barrier\n" \
    "s_setprio 0\n" \
    "v_exp_f32 v96, v96\n" \
    "v_exp_f32 v97, v97\n" \
    "v_smfmac_f32_32x32x32_bf16 v[80:95], v[148:151], v[120:127], v47\n" \
    "ds_read_b128 v[136:139], %[ka] offset:16896\n" \
    "ds_read_b128 v[144:147], %[va] offset:16896\n" \
    "ds_read_b128 v[148:151], %[va] offset:17440\n" \
    "v_exp_f32 v98, v98\n" \
    "v_exp_f32 v99, v99\n" \
    "v_exp_f32 v100, v100\n" \
    "v_mfma_f32_32x32x16_bf16 v[112:127], v[140:143], v[132:135], 0\n" \
    "v_exp_f32 v101, v101\n" \
    "v_exp_f32 v102, v102\n" \
    "v_exp_f32 v103, v103\n" \
    "v_exp_f32 v104, v104\n" \
    "v_exp_f32 v105, v105\n" \
    "v_smfmac_f32_32x32x32_bf16 v[64:79], v[152:155], v[96:103], v47\n" \
    "v_exp_f32 v106, v106\n" \
    "v_exp_f32 v107, v107\n" \
    "v_exp_f32 v108, v108\n" \
    "v_exp_f32 v109, v109\n" \
    "v_exp_f32 v110, v110\n" \
    "v_exp_f32 v111, v111\n" \
    "v_exp_f32 v112, v112\n" \
    "v_exp_f32 v113, v113\n" \
    "v_smfmac_f32_32x32x32_bf16 v[64:79], v[156:159], v[104:111], v47\n" \
    "v_exp_f32 v114, v114\n" \
    "v_exp_f32 v115, v115\n" \
    "v_exp_f32 v116, v116\n" \
    "s_waitcnt lgkmcnt(0)\n" \
    "v_mfma_f32_32x32x16_bf16 v[96:111], v[136:139], v[128:131], 0\n" \
    "v_exp_f32 v117, v117\n" \
    "v_exp_f32 v118, v118\n" \
    "v_exp_f32 v119, v119\n" \
    "v_exp_f32 v120, v120\n" \
    "v_exp_f32 v121, v121\n" \
    "v_smfmac_f32_32x32x32_bf16 v[80:95], v[152:155], v[112:119], v47\n" \
    "v_exp_f32 v122, v122\n" \
    "v_exp_f32 v123, v123\n" \
    "v_exp_f32 v124, v124\n" \
    "v_exp_f32 v125, v125\n" \
    "v_exp_f32 v126, v126\n" \
    "v_exp_f32 v127, v127\n" \
    "s_barrier\n" \
    "s_sub_u32 s60, %[w], 2\n" \
    "s_cmp_lt_u32 s60, %[nft]\n" \
    "s_waitcnt vmcnt(0)\n" \
    "s_cbranch_scc0 7f\n" \
    "s_mov_b64 exec, 1\n" \
    "buffer_store_dword %[tag], off, %[rs], %[hfo] sc1\n" \
    "s_mov_b64 exec, -1\n" \
    "7:\n" \
    "s_setprio 3\n" \
    "v_exp_f32 v96, v96\n" \
    "v_exp_f32 v97, v97\n" \
    "v_smfmac_f32_32x32x32_bf16 v[80:95], v[156:159], v[120:127], v47\n" \
    "ds_read_b128 v[140:143], %[ka] offset:17920\n" \
    "ds_read_b128 v[152:155], %[va] offset:17984\n" \
    "ds_read_b128 v[156:159], %[va] offset:18528\n" \
    "v_exp_f32 v98, v98\n" \
    "v_exp_f32 v99, v99\n" \
    "v_exp_f32 v100, v100\n" \
    "v_mfma_f32_32x32x16_bf16 v[112:127], v[136:139], v[132:135], 0\n" \
    "s_add_u32 s60, %[w2l], 0\n" \
    "s_addc_u32 s61, %[w2h], 0\n" \
    "s_add_u32 m0, %[wdst], 58880\n" \
    "s_nop 0\n" \
    "global_load_lds_dwordx4 %[w3off], s[60:61]\n" \
    "v_exp_f32 v101, v101\n" \
    "v_exp_f32 v102, v102\n" \
    "v_exp_f32 v103, v103\n" \
    "v_exp_f32 v104, v104\n" \
    "v_exp_f32 v105, v105\n" \
    "v_smfmac_f32_32x32x32_bf16 v[64:79], v[144:147], v[96:103], v47\n" \
    "v_exp_f32 v106, v106\n" \
    "v_exp_f32 v107, v107\n" \
    "v_exp_f32 v108, v108\n" \
    "v_exp_f32 v109, v109\n" \
    "v_exp_f32 v110, v110\n" \
    "v_exp_f32 v111, v111\n" \
    "v_exp_f32 v112, v112\n" \
    "v_exp_f32 v113, v113\n" \
    "v_smfmac_f32_32x32x32_bf16 v[64:79], v[148:151], v[104:111], v47\n" \
    "s_add_u32 s60, %[w2l], 16384\n" \
    "s_addc_u32 s61, %[w2h], 0\n" \
    "s_add_u32 m0, %[wdst], 60928\n" \
    "s_nop 0\n" \
    "global_load_lds_dwordx4 %[w3off], s[60:61]\n" \
    "v_exp_f32 v114, v114\n" \
    "v_exp_f32 v115, v115\n" \
    "v_exp_f32 v116, v116\n" \
    "s_waitcnt lgkmcnt(0)\n" \
    "v_mfma_f32_32x32x16_bf16 v[96:111], v[140:143], v[128:131], 0\n" \
    "ds_write_b128 %[kw], v[176:179] offset:0\n" \
    "v_exp_f32 v117, v117\n" \
    "v_exp_f32 v118, v118\n" \
    "v_exp_f32 v119, v119\n" \
    "v_exp_f32 v120, v120\n" \
    "v_exp_f32 v121, v121\n" \
    "v_smfmac_f32_32x32x32_bf16 v[80:95], v[144:147], v[112:119], v47\n" \
    "v_exp_f32 v122, v122\n" \
    "v_exp_f32 v123, v123\n" \
    "v_exp_f32 v124, v124\n" \
    "v_exp_f32 v125, v125\n" \
    "v_exp_f32 v126, v126\n" \
    "v_exp_f32 v127, v127\n" \
    "s_setprio 2\n" \
    "v_exp_f32 v96, v96\n" \
    "v_exp_f32 v97, v97\n" \
    "v_smfmac_f32_32x32x32_bf16 v[80:95], v[148:151], v[120:127], v47\n" \
    "ds_read_b128 v[136:139], %[ka] offset:18944\n" \
    "ds_read_b128 v[144:147], %[va] offset:19072\n" \
    "ds_read_b128 v[148:151], %[va] offset:19616\n" \
    "v_exp_f32 v98, v98\n" \
    "v_exp_f32 v99, v99\n" \
    "v_exp_f32 v100, v100\n" \
    "v_mfma_f32_32x32x16_bf16 v[112:127], v[140:143], v[132:135], 0\n" \
    "v_exp_f32 v101, v101\n" \
    "v_exp_f32 v102, v102\n" \
    "v_exp_f32 v103, v103\n" \
    "v_exp_f32 v104, v104\n" \
    "v_exp_f32 v105, v105\n" \
    "v_smfmac_f32_32x32x32_bf16 v[64:79], v[152:155], v[96:103], v47\n" \
    "s_add_u32 s60, %[w2l], 32768\n" \
    "s_addc_u32 s61, %[w2h], 0\n" \
    "s_add_u32 m0, %[wdst], 62976\n" \
    "s_nop 0\n" \
    "global_load_lds_dwordx4 %[w3off], s[60:61]\n" \
    "v_exp_f32 v106, v106\n" \
    "v_exp_f32 v107, v107\n" \
    "v_exp_f32 v108, v108\n" \
    "v_exp_f32 v109, v109\n" \
    "v_exp_f32 v110, v110\n" \
    "v_exp_f32 v111, v111\n" \
    "v_exp_f32 v112, v112\n" \
    "v_exp_f32 v113, v113\n" \
    "v_smfmac_f32_32x32x32_bf16 v[64:79], v[156:159], v[104:111], v47\n" \
    "v_exp_f32 v114, v114\n" \
    "v_exp_f32 v115, v115\n" \
    "v_exp_f32 v116, v116\n" \
    "s_waitcnt lgkmcnt(0)\n" \
    "v_mfma_f32_32x32x16_bf16 v[96:111], v[136:139], v[128:131], 0\n" \
    "ds_write_b128 %[vw], v[180:183] offset:0\n" \
    "v_exp_f32 v117, v117\n" \
    "v_exp_f32 v118, v118\n" \
    "v_exp_f32 v119, v119\n" \
    "v_exp_f32 v120, v120\n" \
    "v_exp_f32 v121, v121\n" \
    "v_smfmac_f32_32x32x32_bf16 v[80:95], v[152:155], v[112:119], v47\n" \
    "v_exp_f32 v122, v122\n" \
    "v_exp_f32 v123, v123\n" \
    "v_exp_f32 v124, v124\n" \
    "v_exp_f32 v125, v125\n" \
    "v_exp_f32 v126, v126\n" \
    "v_exp_f32 v127, v127\n" \
    "s_setprio 1\n" \
    "v_exp_f32 v96, v96\n" \
    "v_exp_f32 v97, v97\n" \
    "v_smfmac_f32_32x32x32_bf16 v[80:95], v[156:159], v[120:127], v47\n" \
    "ds_read_b128 v[140:143], %[ka] offset:19968\n" \
    "ds_read_b128 v[152:155], %[va] offset:20160\n" \
    "ds_read_b128 v[156:159], %[va] offset:20704\n" \
    "v_exp_f32 v98, v98\n" \
    "v_exp_f32 v99, v99\n" \
    "v_exp_f32 v100, v100\n" \
    "v_mfma_f32_32x32x16_bf16 v[112:127], v[136:139], v[132:135], 0\n" \
    "s_add_u32 s60, %[w2l], 49152\n" \
    "s_addc_u32 s61, %[w2h], 0\n" \
    "s_add_u32 m0, %[wdst], 83456\n" \
    "s_nop 0\n" \
    "global_load_lds_dwordx4 %[w3off], s[60:61]\n" \
    "v_exp_f32 v101, v101\n" \
    "v_exp_f32 v102, v102\n" \
    "v_exp_f32 v103, v103\n" \
    "v_exp_f32 v104, v104\n" \
    "v_exp_f32 v105, v105\n" \
    "v_smfmac_f32_32x32x32_bf16 v[64:79], v[144:147], v[96:103], v47\n" \
    "v_exp_f32 v106, v106\n" \
    "v_exp_f32 v107, v107\n" \
    "v_exp_f32 v108, v108\n" \
    "v_exp_f32 v109, v109\n" \
    "v_exp_f32 v110, v110\n" \
    "v_exp_f32 v111, v111\n" \
    "v_exp_f32 v112, v112\n" \
    "v_exp_f32 v113, v113\n" \
    "v_smfmac_f32_32x32x32_bf16 v[64:79], v[148:151], v[104:111], v47\n" \
    "s_add_u32 s60, %[w2l], 65536\n" \
    "s_addc_u32 s61, %[w2h], 0\n" \
    "s_add_u32 m0, %[wdst], 85504\n" \
    "s_nop 0\n" \
    "global_load_lds_dwordx4 %[w3off], s[60:61]\n" \
    "v_exp_f32 v114, v114\n" \
    "v_exp_f32 v115, v115\n" \
    "v_exp_f32 v116, v116\n" \
    "s_waitcnt lgkmcnt(0)\n" \
    "v_mfma_f32_32x32x16_bf16 v[96:111], v[140:143], v[128:131], 0\n" \
    "v_exp_f32 v117, v117\n" \
    "v_exp_f32 v118, v118\n" \
    "v_exp_f32 v119, v119\n" \
    "v_exp_f32 v120, v120\n" \
    "v_exp_f32 v121, v121\n" \
    "v_smfmac_f32_32x32x32_bf16 v[80:95], v[144:147], v[112:119], v47\n" \
    "v_exp_f32 v122, v122\n" \
    "v_exp_f32 v123, v123\n" \
    "v_exp_f32 v124, v124\n" \
    "v_exp_f32 v125, v125\n" \
    "v_exp_f32 v126, v126\n" \
    "v_exp_f32 v127, v127\n" \
    "s_setprio 0\n" \
    "v_exp_f32 v96, v96\n" \
    "v_exp_f32 v97, v97\n" \
    "v_smfmac_f32_32x32x32_bf16 v[80:95], v[148:151], v[120:127], v47\n" \
    "ds_read_b128 v[136:139], %[ka] offset:33792\n" \
    "ds_read_b128 v[144:147], %[va] offset:33792\n" \
    "ds_read_b128 v[148:151], %[va] offset:34336\n" \
    "v_exp_f32 v98, v98\n" \
    "v_exp_f32 v99, v99\n" \
    "v_exp_f32 v100, v100\n" \
    "v_mfma_f32_32x32x16_bf16 v[112:127], v[140:143], v[132:135], 0\n" \
    "v_exp_f32 v101, v101\n" \
    "v_exp_f32 v102, v102\n" \
    "v_exp_f32 v103, v103\n" \
    "v_exp_f32 v104, v104\n" \
    "v_exp_f32 v105, v105\n" \
    "v_smfmac_f32_32x32x32_bf16 v[64:79], v[152:155], v[96:103], v47\n" \
    "s_add_u32 s60, %[w2l], 81920\n" \
    "s_addc_u32 s61, %[w2h], 0\n" \
    "s_add_u32 m0, %[wdst], 87552\n" \
    "s_nop 0\n" \
    "global_load_lds_dwordx4 %[w3off], s[60:61]\n" \
    "v_exp_f32 v106, v106\n" \
    "v_exp_f32 v107, v107\n" \
    "v_exp_f32 v108, v108\n" \
    "v_exp_f32 v109, v109\n" \
    "v_exp_f32 v110, v110\n" \
    "v_exp_f32 v111, v111\n" \
    "v_exp_f32 v112, v112\n" \
    "v_exp_f32 v113, v113\n" \
    "v_smfmac_f32_32x32x32_bf16 v[64:79], v[156:159], v[104:111], v47\n" \
    "v_exp_f32 v114, v114\n" \
    "v_exp_f32 v115, v115\n" \
    "v_exp_f32 v116, v116\n" \
    "s_waitcnt lgkmcnt(0)\n" \
    "v_mfma_f32_32x32x16_bf16 v[96:111], v[136:139], v[128:131], 0\n" \
    "v_exp_f32 v117, v117\n" \
    "v_exp_f32 v118, v118\n" \
    "v_exp_f32 v119, v119\n" \
    "v_exp_f32 v120, v120\n" \
    "v_exp_f32 v121, v121\n" \
    "v_smfmac_f32_32x32x32_bf16 v[80:95], v[152:155], v[112:119], v47\n" \
    "v_exp_f32 v122, v122\n" \
    "v_exp_f32 v123, v123\n" \
    "v_exp_f32 v124, v124\n" \
    "v_exp_f32 v125, v125\n" \
    "v_exp_f32 v126, v126\n" \
    "v_exp_f32 v127, v127\n" \
    "s_barrier\n" \
    "s_setprio 3\n" \
    "v_exp_f32 v96, v96\n" \
    "v_exp_f32 v97, v97\n" \
    "v_smfmac_f32_32x32x32_bf16 v[80:95], v[156:159], v[120:127], v47\n" \
    "ds_read_b128 v[140:143], %[ka] offset:34816\n" \
    "ds_read_b128 v[152:155], %[va] offset:34880\n" \
    "ds_read_b128 v[156:159], %[va] offset:35424\n" \
    "v_exp_f32 v98, v98\n" \
    "v_exp_f32 v99, v99\n" \
    "v_exp_f32 v100, v100\n" \
    "v_mfma_f32_32x32x16_bf16 v[112:127], v[136:139], v[132:135], 0\n" \
    "s_add_u32 s60, %[w2l], 98304\n" \
    "s_addc_u32 s61, %[w2h], 0\n" \
    "s_add_u32 m0, %[wdst], 108032\n" \
    "s_nop 0\n" \
    "global_load_lds_dwordx4 %[w3off], s[60:61]\n" \
    "v_exp_f32 v101, v101\n" \
    "v_exp_f32 v102, v102\n" \
    "v_exp_f32 v103, v103\n" \
    "v_exp_f32 v104, v104\n" \
    "v_exp_f32 v105, v105\n" \
    "v_smfmac_f32_32x32x32_bf16 v[64:79], v[144:147], v[96:103], v47\n" \
    "s_add_u32 s60, %[w2l], 114688\n" \
    "s_addc_u32 s61, %[w2h], 0\n" \
    "s_add_u32 m0, %[wdst], 110080\n" \
    "s_nop 0\n" \
    "global_load_lds_dwordx4 %[w3off], s[60:61]\n" \
    "v_exp_f32 v106, v106\n" \
    "v_exp_f32 v107, v107\n" \
    "v_exp_f32 v108, v108\n" \
    "v_exp_f32 v109, v109\n" \
    "v_exp_f32 v110, v110\n" \
    "v_exp_f32 v111, v111\n" \
    "v_exp_f32 v112, v112\n" \
    "v_exp_f32 v113, v113\n" \
    "v_smfmac_f32_32x32x32_bf16 v[64:79], v[148:151], v[104:111], v47\n" \
    "v_exp_f32 v114, v114\n" \
    "v_exp_f32 v115, v115\n" \
    "v_exp_f32 v116, v116\n" \
    "s_waitcnt lgkmcnt(0)\n" \
    "v_mfma_f32_32x32x16_bf16 v[96:111], v[140:143], v[128:131], 0\n" \
    "ds_write_b128 %[kw], v[184:187] offset:16896\n" \
    "v_exp_f32 v117, v117\n" \
    "v_exp_f32 v118, v118\n" \
    "v_exp_f32 v119, v119\n" \
    "v_exp_f32 v120, v120\n" \
    "v_exp_f32 v121, v121\n" \
    "v_smfmac_f32_32x32x32_bf16 v[80:95], v[144:147], v[112:119], v47\n" \
    "v_exp_f32 v122, v122\n" \
    "v_exp_f32 v123, v123\n" \
    "v_exp_f32 v124, v124\n" \
    "v_exp_f32 v125, v125\n" \
    "v_exp_f32 v126, v126\n" \
    "v_exp_f32 v127, v127\n" \
    "s_setprio 2\n" \
    "v_exp_f32 v96, v96\n" \
    "v_exp_f32 v97, v97\n" \
    "v_smfmac_f32_32x32x32_bf16 v[80:95], v[148:151], v[120:127], v47\n" \
    "ds_read_b128 v[136:139], %[ka] offset:35840\n" \
    "ds_read_b128 v[144:147], %[va] offset:35968\n" \
    "ds_read_b128 v[148:151], %[va] offset:36512\n" \
    "v_exp_f32 v98, v98\n" \
    "v_exp_f32 v99, v99\n" \
    "v_exp_f32 v100, v100\n" \
    "v_mfma_f32_32x32x16_bf16 v[112:127], v[140:143], v[132:135], 0\n" \
    "s_add_u32 s60, %[w2l], 131072\n" \
    "s_addc_u32 s61, %[w2h], 0\n" \
    "s_add_u32 m0, %[wdst], 112128\n" \
    "s_nop 0\n" \
    "global_load_lds_dwordx4 %[w3off], s[60:61]\n" \
    "v_exp_f32 v101, v101\n" \
    "v_exp_f32 v102, v102\n" \
    "v_exp_f32 v103, v103\n" \
    "v_exp_f32 v104, v104\n" \
    "v_exp_f32 v105, v105\n" \
    "v_smfmac_f32_32x32x32_bf16 v[64:79], v[152:155], v[96:103], v47\n" \
    "buffer_load_dword v46, %[hflo], %[rs], 0 offen sc1\n" \
    "v_exp_f32 v106, v106\n" \
    "v_exp_f32 v107, v107\n" \
    "v_exp_f32 v108, v108\n" \
    "v_exp_f32 v109, v109\n" \
    "v_exp_f32 v110, v110\n" \
    "v_exp_f32 v111, v111\n" \
    "v_exp_f32 v112, v112\n" \
    "v_exp_f32 v113, v113\n" \
    "v_smfmac_f32_32x32x32_bf16 v[64:79], v[156:159], v[104:111], v47\n" \
    "v_exp_f32 v114, v114\n" \
    "v_exp_f32 v115, v115\n" \
    "v_exp_f32 v116, v116\n" \
    "s_waitcnt lgkmcnt(0)\n" \
    "v_mfma_f32_32x32x16_bf16 v[96:111], v[136:139], v[128:131], 0\n" \
    "ds_write_b128 %[vw], v[188:191] offset:16896\n" \
    "v_exp_f32 v117, v117\n" \
    "v_exp_f32 v118, v118\n" \
    "v_exp_f32 v119, v119\n" \
    "v_exp_f32 v120, v120\n" \
    "v_exp_f32 v121, v121\n" \
    "v_smfmac_f32_32x32x32_bf16 v[80:95], v[152:155], v[112:119], v47\n" \
    "v_exp_f32 v122, v122\n" \
    "v_exp_f32 v123, v123\n" \
    "v_exp_f32 v124, v124\n" \
    "v_exp_f32 v125, v125\n" \
    "v_exp_f32 v126, v126\n" \
    "v_exp_f32 v127, v127\n" \
    "s_setprio 1\n" \
    "v_exp_f32 v96, v96\n" \
    "v_exp_f32 v97, v97\n" \
    "v_smfmac_f32_32x32x32_bf16 v[80:95], v[156:159], v[120:127], v47\n" \
    "ds_read_b128 v[140:143], %[ka] offset:36864\n" \
    "ds_read_b128 v[152:155], %[va] offset:37056\n" \
    "ds_read_b128 v[156:159], %[va] offset:37600\n" \
    "v_exp_f32 v98, v98\n" \
    "v_exp_f32 v99, v99\n" \
    "v_exp_f32 v100, v100\n" \
    "v_mfma_f32_32x32x16_bf16 v[112:127], v[136:139], v[132:135], 0\n" \
    "s_add_u32 s60, %[w2l], 147456\n" \
    "s_addc_u32 s61, %[w2h], 0\n" \
    "s_add_u32 m0, %[wdst], 132608\n" \
    "s_nop 0\n" \
    "global_load_lds_dwordx4 %[w3off], s[60:61]\n" \
    "v_exp_f32 v101, v101\n" \
    "v_exp_f32 v102, v102\n" \
    "v_exp_f32 v103, v103\n" \
    "v_exp_f32 v104, v104\n" \
    "v_exp_f32 v105, v105\n" \
    "v_smfmac_f32_32x32x32_bf16 v[64:79], v[144:147], v[96:103], v47\n" \
    "s_add_u32 s60, %[w2l], 163840\n" \
    "s_addc_u32 s61, %[w2h], 0\n" \
    "s_add_u32 m0, %[wdst], 134656\n" \
    "s_nop 0\n" \
    "global_load_lds_dwordx4 %[w3off], s[60:61]\n" \
    "v_exp_f32 v106, v106\n" \
    "v_exp_f32 v107, v107\n" \
    "v_exp_f32 v108, v108\n" \
    "v_exp_f32 v109, v109\n" \
    "v_exp_f32 v110, v110\n" \
    "v_exp_f32 v111, v111\n" \
    "v_exp_f32 v112, v112\n" \
    "v_exp_f32 v113, v113\n" \
    "v_smfmac_f32_32x32x32_bf16 v[64:79], v[148:151], v[104:111], v47\n" \
    "v_exp_f32 v114, v114\n" \
    "v_exp_f32 v115, v115\n" \
    "v_exp_f32 v116, v116\n" \
    "s_waitcnt lgkmcnt(0)\n" \
    "v_mfma_f32_32x32x16_bf16 v[96:111], v[140:143], v[128:131], 0\n" \
    "v_exp_f32 v117, v117\n" \
    "v_exp_f32 v118, v118\n" \
    "v_exp_f32 v119, v119\n" \
    "v_exp_f32 v120, v120\n" \
    "v_exp_f32 v121, v121\n" \
    "v_smfmac_f32_32x32x32_bf16 v[80:95], v[144:147], v[112:119], v47\n" \
    "v_exp_f32 v122, v122\n" \
    "v_exp_f32 v123, v123\n" \
    "v_exp_f32 v124, v124\n" \
    "v_exp_f32 v125, v125\n" \
    "v_exp_f32 v126, v126\n" \
    "v_exp_f32 v127, v127\n" \
    "s_setprio 0\n" \
    "v_exp_f32 v96, v96\n" \
    "v_exp_f32 v97, v97\n" \
    "v_smfmac_f32_32x32x32_bf16 v[80:95], v[148:151], v[120:127], v47\n" \
    "ds_read_b128 v[136:139], %[ka] offset:0\n" \
    "ds_read_b128 v[144:147], %[va] offset:0\n" \
    "ds_read_b128 v[148:151], %[va] offset:544\n" \
    "v_exp_f32 v98, v98\n" \
    "v_exp_f32 v99, v99\n" \
    "v_exp_f32 v100, v100\n" \
    "v_mfma_f32_32x32x16_bf16 v[112:127], v[140:143], v[132:135], 0\n" \
    "s_add_u32 s60, %[w2l], 180224\n" \
    "s_addc_u32 s61, %[w2h], 0\n" \
    "s_add_u32 m0, %[wdst], 136704\n" \
    "s_nop 0\n" \
    "global_load_lds_dwordx4 %[w3off], s[60:61]\n" \
    "v_exp_f32 v101, v101\n" \
    "v_exp_f32 v102, v102\n" \
    "v_exp_f32 v103, v103\n" \
    "v_exp_f32 v104, v104\n" \
    "v_exp_f32 v105, v105\n" \
    "v_smfmac_f32_32x32x32_bf16 v[64:79], v[152:155], v[96:103], v47\n" \
    "s_mov_b32 m0, %[scr]\n" \
    "s_nop 0\n" \
    "global_load_lds_dword %[pfp], off\n" \
    "v_exp_f32 v106, v106\n" \
    "v_exp_f32 v107, v107\n" \
    "v_exp_f32 v108, v108\n" \
    "v_exp_f32 v109, v109\n" \
    "v_exp_f32 v110, v110\n" \
    "v_exp_f32 v111, v111\n" \
    "v_exp_f32 v112, v112\n" \
    "v_exp_f32 v113, v113\n" \
    "v_smfmac_f32_32x32x32_bf16 v[64:79], v[156:159], v[104:111], v47\n" \
    "v_exp_f32 v114, v114\n" \
    "v_exp_f32 v115, v115\n" \
    "v_exp_f32 v116, v116\n" \
    "s_waitcnt lgkmcnt(0)\n" \
    "v_mfma_f32_32x32x16_bf16 v[96:111], v[136:139], v[128:131], 0\n" \
    "v_exp_f32 v117, v117\n" \
    "v_exp_f32 v118, v118\n" \
    "v_exp_f32 v119, v119\n" \
    "v_exp_f32 v120, v120\n" \
    "v_exp_f32 v121, v121\n" \
    "v_smfmac_f32_32x32x32_bf16 v[80:95], v[152:155], v[112:119], v47\n" \
    "v_exp_f32 v122, v122\n" \
    "v_exp_f32 v123, v123\n" \
    "v_exp_f32 v124, v124\n" \
    "v_exp_f32 v125, v125\n" \
    "v_exp_f32 v126, v126\n" \
    "v_exp_f32 v127, v127\n" \
    "s_barrier\n" \
    "s_setprio 3\n" \
    "v_exp_f32 v96, v96\n" \
    "v_exp_f32 v97, v97\n" \
    "v_smfmac_f32_32x32x32_bf16 v[80:95], v[156:159], v[120:127], v47\n" \
    "ds_read_b128 v[140:143], %[ka] offset:1024\n" \
    "ds_read_b128 v[152:155], %[va] offset:1088\n" \
    "ds_read_b128 v[156:159], %[va] offset:1632\n" \
    "v_exp_f32 v98, v98\n" \
    "v_exp_f32 v99, v99\n" \
    "v_exp_f32 v100, v100\n" \
    "v_mfma_f32_32x32x16_bf16 v[112:127], v[136:139], v[132:135], 0\n" \
    "s_waitcnt vmcnt(4)\n" \
    "s_mov_b32 s61, 0x40000\n" \
    "2:\n" \
    "v_cmp_ne_u32 vcc, v46, %[tag]\n" \
    "s_nop 1\n" \
    "s_and_b64 vcc, vcc, %[hmask]\n" \
    "s_cbranch_scc0 3f\n" \
    "s_sleep 2\n" \
    "buffer_load_dword v46, %[hflo], %[rs], 0 offen sc1\n" \
    "s_sub_u32 s61, s61, 1\n" \
    "s_waitcnt vmcnt(0)\n" \
    "s_cbranch_scc0 2b\n" \
    "3:\n" \
    "v_exp_f32 v101, v101\n" \
    "v_exp_f32 v102, v102\n" \
    "v_exp_f32 v103, v103\n" \
    "v_exp_f32 v104, v104\n" \
    "v_exp_f32 v105, v105\n" \
    "v_smfmac_f32_32x32x32_bf16 v[64:79], v[144:147], v[96:103], v47\n" \
    "s_movk_i32 s60, 0\n" \
    "buffer_load_dwordx4 v[160:163], %[hgo], %[rs], s60 offen offset:0 sc1\n" \
    "v_exp_f32 v106, v106\n" \
    "v_exp_f32 v107, v107\n" \
    "v_exp_f32 v108, v108\n" \
    "v_exp_f32 v109, v109\n" \
    "v_exp_f32 v110, v110\n" \
    "v_exp_f32 v111, v111\n" \
    "v_exp_f32 v112, v112\n" \
    "v_exp_f32 v113, v113\n" \
    "v_smfmac_f32_32x32x32_bf16 v[64:79], v[148:151], v[104:111], v47\n" \
    "s_movk_i32 s60, 0\n" \
    "buffer_load_dwordx4 v[164:167], %[hgo], %[rs], s60 offen offset:1024 sc1\n" \
    "v_exp_f32 v114, v114\n" \
    "v_exp_f32 v115, v115\n" \
    "v_exp_f32 v116, v116\n" \
    "s_waitcnt lgkmcnt(0)\n" \
    "v_mfma_f32_32x32x16_bf16 v[96:111], v[140:143], v[128:131], 0\n" \
    "ds_write_b128 %[kw], v[224:227] offset:33792\n" \
    "v_exp_f32 v117, v117\n" \
    "v_exp_f32 v118, v118\n" \
    "v_exp_f32 v119, v119\n" \
    "v_exp_f32 v120, v120\n" \
    "v_exp_f32 v121, v121\n" \
    "v_smfmac_f32_32x32x32_bf16 v[80:95], v[144:147], v[112:119], v47\n" \
    "v_exp_f32 v122, v122\n" \
    "v_exp_f32 v123, v123\n" \
    "v_exp_f32 v124, v124\n" \
    "v_exp_f32 v125, v125\n" \
    "v_exp_f32 v126, v126\n" \
    "v_exp_f32 v127, v127\n" \
    "s_setprio 2\n" \
    "v_exp_f32 v96, v96\n" \
    "v_exp_f32 v97, v97\n" \
    "v_smfmac_f32_32x32x32_bf16 v[80:95], v[148:151], v[120:127], v47\n" \
    "ds_read_b128 v[136:139], %[ka] offset:2048\n" \
    "ds_read_b128 v[144:147], %[va] offset:2176\n" \
    "ds_read_b128 v[148:151], %[va] offset:2720\n" \
    "v_exp_f32 v98, v98\n" \
    "v_exp_f32 v99, v99\n" \
    "v_exp_f32 v100, v100\n" \
    "v_mfma_f32_32x32x16_bf16 v[112:127], v[140:143], v[132:135], 0\n" \
    "v_exp_f32 v101, v101\n" \
    "v_exp_f32 v102, v102\n" \
    "v_exp_f32 v103, v103\n" \
    "v_exp_f32 v104, v104\n" \
    "v_exp_f32 v105, v105\n" \
    "v_smfmac_f32_32x32x32_bf16 v[64:79], v[152:155], v[96:103], v47\n" \
    "s_movk_i32 s60, 0\n" \
    "buffer_load_dwordx4 v[168:171], %[hgo], %[rs], s60 offen offset:2048 sc1\n" \
    "v_exp_f32 v106, v106\n" \
    "v_exp_f32 v107, v107\n" \
    "v_exp_f32 v108, v108\n" \
    "v_exp_f32 v109, v109\n" \
    "v_exp_f32 v110, v110\n" \
    "v_exp_f32 v111, v111\n" \
    "v_exp_f32 v112, v112\n" \
    "v_exp_f32 v113, v113\n" \
    "v_smfmac_f32_32x32x32_bf16 v[64:79], v[156:159], v[104:111], v47\n" \
    "s_movk_i32 s60, 0\n" \
    "buffer_load_dwordx4 v[172:175], %[hgo], %[rs], s60 offen offset:3072 sc1\n" \
    "v_exp_f32 v114, v114\n" \
    "v_exp_f32 v115, v115\n" \
    "v_exp_f32 v116, v116\n" \
    "s_waitcnt lgkmcnt(0)\n" \
    "v_mfma_f32_32x32x16_bf16 v[96:111], v[136:139], v[128:131], 0\n" \
    "ds_write_b128 %[vw], v[228:231] offset:33792\n" \
    "v_exp_f32 v117, v117\n" \
    "v_exp_f32 v118, v118\n" \
    "v_exp_f32 v119, v119\n" \
    "v_exp_f32 v120, v120\n" \
    "v_exp_f32 v121, v121\n" \
    "v_smfmac_f32_32x32x32_bf16 v[80:95], v[152:155], v[112:119], v47\n" \
    "v_exp_f32 v122, v122\n" \
    "v_exp_f32 v123, v123\n" \
    "v_exp_f32 v124, v124\n" \
    "v_exp_f32 v125, v125\n" \
    "v_exp_f32 v126, v126\n" \
    "v_exp_f32 v127, v127\n" \
    "s_setprio 1\n" \
    "v_exp_f32 v96, v96\n" \
    "v_exp_f32 v97, v97\n" \
    "v_smfmac_f32_32x32x32_bf16 v[80:95], v[156:159], v[120:127], v47\n" \
    "ds_read_b128 v[140:143], %[ka] offset:3072\n" \
    "ds_read_b128 v[152:155], %[va] offset:3264\n" \
    "ds_read_b128 v[156:159], %[va] offset:3808\n" \
    "v_exp_f32 v98, v98\n" \
    "v_exp_f32 v99, v99\n" \
    "v_exp_f32 v100, v100\n" \
    "v_mfma_f32_32x32x16_bf16 v[112:127], v[136:139], v[132:135], 0\n" \
    "v_exp_f32 v101, v101\n" \
    "v_exp_f32 v102, v102\n" \
    "v_exp_f32 v103, v103\n" \
    "v_exp_f32 v104, v104\n" \
    "v_exp_f32 v105, v105\n" \
    "v_smfmac_f32_32x32x32_bf16 v[64:79], v[144:147], v[96:103], v47\n" \
    "s_movk_i32 s60, 8192\n" \
    "buffer_load_dwordx4 v[192:195], %[hgo], %[rs], s60 offen offset:0 sc1\n" \
    "v_exp_f32 v106, v106\n" \
    "v_exp_f32 v107, v107\n" \
    "v_exp_f32 v108, v108\n" \
    "v_exp_f32 v109, v109\n" \
    "v_exp_f32 v110, v110\n" \
    "v_exp_f32 v111, v111\n" \
    "v_exp_f32 v112, v112\n" \
    "v_exp_f32 v113, v113\n" \
    "v_smfmac_f32_32x32x32_bf16 v[64:79], v[148:151], v[104:111], v47\n" \
    "s_movk_i32 s60, 8192\n" \
    "buffer_load_dwordx4 v[196:199], %[hgo], %[rs], s60 offen offset:1024 sc1\n" \
    "v_exp_f32 v114, v114\n" \
    "v_exp_f32 v115, v115\n" \
    "v_exp_f32 v116, v116\n" \
    "s_waitcnt lgkmcnt(0)\n" \
    "v_mfma_f32_32x32x16_bf16 v[96:111], v[140:143], v[128:131], 0\n" \
    "v_exp_f32 v117, v117\n" \
    "v_exp_f32 v118, v118\n" \
    "v_exp_f32 v119, v119\n" \
    "v_exp_f32 v120, v120\n" \
    "v_exp_f32 v121, v121\n" \
    "v_smfmac_f32_32x32x32_bf16 v[80:95], v[144:147], v[112:119], v47\n" \
    "v_exp_f32 v122, v122\n" \
    "v_exp_f32 v123, v123\n" \
    "v_exp_f32 v124, v124\n" \
    "v_exp_f32 v125, v125\n" \
    "v_exp_f32 v126, v126\n" \
    "v_exp_f32 v127, v127\n" \
    "s_setprio 0\n" \
    "v_exp_f32 v96, v96\n" \
    "v_exp_f32 v97, v97\n" \
    "v_smfmac_f32_32x32x32_bf16 v[80:95], v[148:151], v[120:127], v47\n" \
    "ds_read_b128 v[136:139], %[ka] offset:16896\n" \
    "ds_read_b128 v[144:147], %[va] offset:16896\n" \
    "ds_read_b128 v[148:151], %[va] offset:17440\n" \
    "v_exp_f32 v98, v98\n" \
    "v_exp_f32 v99, v99\n" \
    "v_exp_f32 v100, v100\n" \
    "v_mfma_f32_32x32x16_bf16 v[112:127], v[140:143], v[132:135], 0\n" \
    "v_exp_f32 v101, v101\n" \
    "v_exp_f32 v102, v102\n" \
    "v_exp_f32 v103, v103\n" \
    "v_exp_f32 v104, v104\n" \
    "v_exp_f32 v105, v105\n" \
    "v_smfmac_f32_32x32x32_bf16 v[64:79], v[152:155], v[96:103], v47\n" \
    "s_movk_i32 s60, 8192\n" \
    "buffer_load_dwordx4 v[200:203], %[hgo], %[rs], s60 offen offset:2048 sc1\n" \
    "v_exp_f32 v106, v106\n" \
    "v_exp_f32 v107, v107\n" \
    "v_exp_f32 v108, v108\n" \
    "v_exp_f32 v109, v109\n" \
    "v_exp_f32 v110, v110\n" \
    "v_exp_f32 v111, v111\n" \
    "v_exp_f32 v112, v112\n" \
    "v_exp_f32 v113, v113\n" \
    "v_smfmac_f32_32x32x32_bf16 v[64:79], v[156:159], v[104:111], v47\n" \
    "v_exp_f32 v114, v114\n" \
    "v_exp_f32 v115, v115\n" \
    "v_exp_f32 v116, v116\n" \
    "s_waitcnt lgkmcnt(0)\n" \
    "v_mfma_f32_32x32x16_bf16 v[96:111], v[136:139], v[128:131], 0\n" \
    "v_exp_f32 v117, v117\n" \
    "v_exp_f32 v118, v118\n" \
    "v_exp_f32 v119, v119\n" \
    "v_exp_f32 v120, v120\n" \
    "v_exp_f32 v121, v121\n" \
    "v_smfmac_f32_32x32x32_bf16 v[80:95], v[152:155], v[112:119], v47\n" \
    "v_exp_f32 v122, v122\n" \
    "v_exp_f32 v123, v123\n" \
    "v_exp_f32 v124, v124\n" \
    "v_exp_f32 v125, v125\n" \
    "v_exp_f32 v126, v126\n" \
    "v_exp_f32 v127, v127\n" \
    "s_barrier\n" \
    "s_setprio 3\n" \
    "v_exp_f32 v96, v96\n" \
    "v_exp_f32 v97, v97\n" \
    "v_smfmac_f32_32x32x32_bf16 v[80:95], v[156:159], v[120:127], v47\n" \
    "ds_read_b128 v[140:143], %[ka] offset:17920\n" \
    "ds_read_b128 v[152:155], %[va] offset:17984\n" \
    "ds_read_b128 v[156:159], %[va] offset:18528\n" \
    "v_exp_f32 v98, v98\n" \
    "v_exp_f32 v99, v99\n" \
    "v_exp_f32 v100, v100\n" \
    "v_mfma_f32_32x32x16_bf16 v[112:127], v[136:139], v[132:135], 0\n" \
    "s_movk_i32 s60, 4096\n" \
    "buffer_load_dwordx4 v[176:179], %[hgo], %[rs], s60 offen offset:0 sc1\n" \
    "v_exp_f32 v101, v101\n" \
    "v_exp_f32 v102, v102\n" \
    "v_exp_f32 v103, v103\n" \
    "v_exp_f32 v104, v104\n" \
    "v_exp_f32 v105, v105\n" \
    "v_smfmac_f32_32x32x32_bf16 v[64:79], v[144:147], v[96:103], v47\n" \
    "s_movk_i32 s60, 4096\n" \
    "buffer_load_dwordx4 v[180:183], %[hgo], %[rs], s60 offen offset:1024 sc1\n" \
    "v_exp_f32 v106, v106\n" \
    "v_exp_f32 v107, v107\n" \
    "v_exp_f32 v108, v108\n" \
    "v_exp_f32 v109, v109\n" \
    "v_exp_f32 v110, v110\n" \
    "v_exp_f32 v111, v111\n" \
    "v_exp_f32 v112, v112\n" \
    "v_exp_f32 v113, v113\n" \
    "v_smfmac_f32_32x32x32_bf16 v[64:79], v[148:151], v[104:111], v47\n" \
    "v_exp_f32 v114, v114\n" \
    "v_exp_f32 v115, v115\n" \
    "v_exp_f32 v116, v116\n" \
    "s_waitcnt lgkmcnt(0)\n" \
    "v_mfma_f32_32x32x16_bf16 v[96:111], v[140:143], v[128:131], 0\n" \
    "ds_write_b128 %[kw], v[232:235] offset:0\n" \
    "v_exp_f32 v117, v117\n" \
    "v_exp_f32 v118, v118\n" \
    "v_exp_f32 v119, v119\n" \
    "v_exp_f32 v120, v120\n" \
    "v_exp_f32 v121, v121\n" \
    "v_smfmac_f32_32x32x32_bf16 v[80:95], v[144:147], v[112:119], v47\n" \
    "v_exp_f32 v122, v122\n" \
    "v_exp_f32 v123, v123\n" \
    "v_exp_f32 v124, v124\n" \
    "v_exp_f32 v125, v125\n" \
    "v_exp_f32 v126, v126\n" \
    "v_exp_f32 v127, v127\n" \
    "s_setprio 2\n" \
    "v_exp_f32 v96, v96\n" \
    "v_exp_f32 v97, v97\n" \
    "v_smfmac_f32_32x32x32_bf16 v[80:95], v[148:151], v[120:127], v47\n" \
    "ds_read_b128 v[136:139], %[ka] offset:18944\n" \
    "ds_read_b128 v[144:147], %[va] offset:19072\n" \
    "ds_read_b128 v[148:151], %[va] offset:19616\n" \
    "v_exp_f32 v98, v98\n" \
    "v_exp_f32 v99, v99\n" \
    "v_exp_f32 v100, v100\n" \
    "v_mfma_f32_32x32x16_bf16 v[112:127], v[140:143], v[132:135], 0\n" \
    "s_movk_i32 s60, 8192\n" \
    "buffer_load_dwordx4 v[204:207], %[hgo], %[rs], s60 offen offset:3072 sc1\n" \
    "v_exp_f32 v101, v101\n" \
    "v_exp_f32 v102, v102\n" \
    "v_exp_f32 v103, v103\n" \
    "v_exp_f32 v104, v104\n" \
    "v_exp_f32 v105, v105\n" \
    "v_smfmac_f32_32x32x32_bf16 v[64:79], v[152:155], v[96:103], v47\n" \
    "v_exp_f32 v106, v106\n" \
    "v_exp_f32 v107, v107\n" \
    "v_exp_f32 v108, v108\n" \
    "v_exp_f32 v109, v109\n" \
    "v_exp_f32 v110, v110\n" \
    "v_exp_f32 v111, v111\n" \
    "v_exp_f32 v112, v112\n" \
    "v_exp_f32 v113, v113\n" \
    "v_smfmac_f32_32x32x32_bf16 v[64:79], v[156:159], v[104:111], v47\n" \
    "s_movk_i32 s60, 12288\n" \
    "buffer_load_dwordx4 v[208:211], %[hgo], %[rs], s60 offen offset:0 sc1\n" \
    "v_exp_f32 v114, v114\n" \
    "v_exp_f32 v115, v115\n" \
    "v_exp_f32 v116, v116\n" \
    "s_waitcnt lgkmcnt(0)\n" \
    "v_mfma_f32_32x32x16_bf16 v[96:111], v[136:139], v[128:131], 0\n" \
    "ds_write_b128 %[vw], v[236:239] offset:0\n" \
    "v_exp_f32 v117, v117\n" \
    "v_exp_f32 v118, v118\n" \
    "v_exp_f32 v119, v119\n" \
    "v_exp_f32 v120, v120\n" \
    "v_exp_f32 v121, v121\n" \
    "v_smfmac_f32_32x32x32_bf16 v[80:95], v[152:155], v[112:119], v47\n" \
    "v_exp_f32 v122, v122\n" \
    "v_exp_f32 v123, v123\n" \
    "v_exp_f32 v124, v124\n" \
    "v_exp_f32 v125, v125\n" \
    "v_exp_f32 v126, v126\n" \
    "v_exp_f32 v127, v127\n" \
    "s_setprio 1\n" \
    "v_exp_f32 v96, v96\n" \
    "v_exp_f32 v97, v97\n" \
    "v_smfmac_f32_32x32x32_bf16 v[80:95], v[156:159], v[120:127], v47\n" \
    "ds_read_b128 v[140:143], %[ka] offset:19968\n" \
    "ds_read_b128 v[152:155], %[va] offset:20160\n" \
    "ds_read_b128 v[156:159], %[va] offset:20704\n" \
    "v_exp_f32 v98, v98\n" \
    "v_exp_f32 v99, v99\n" \
    "v_exp_f32 v100, v100\n" \
    "v_mfma_f32_32x32x16_bf16 v[112:127], v[136:139], v[132:135], 0\n" \
    "s_movk_i32 s60, 12288\n" \
    "buffer_load_dwordx4 v[212:215], %[hgo], %[rs], s60 offen offset:1024 sc1\n" \
    "v_exp_f32 v101, v101\n" \
    "v_exp_f32 v102, v102\n" \
    "v_exp_f32 v103, v103\n" \
    "v_exp_f32 v104, v104\n" \
    "v_exp_f32 v105, v105\n" \
    "v_smfmac_f32_32x32x32_bf16 v[64:79], v[144:147], v[96:103], v47\n" \
    "v_exp_f32 v106, v106\n" \
    "v_exp_f32 v107, v107\n" \
    "v_exp_f32 v108, v108\n" \
    "v_exp_f32 v109, v109\n" \
    "v_exp_f32 v110, v110\n" \
    "v_exp_f32 v111, v111\n" \
    "v_exp_f32 v112, v112\n" \
    "v_exp_f32 v113, v113\n" \
    "v_smfmac_f32_32x32x32_bf16 v[64:79], v[148:151], v[104:111], v47\n" \
    "s_movk_i32 s60, 12288\n" \
    "buffer_load_dwordx4 v[216:219], %[hgo], %[rs], s60 offen offset:2048 sc1\n" \
    "v_exp_f32 v114, v114\n" \
    "v_exp_f32 v115, v115\n" \
    "v_exp_f32 v116, v116\n" \
    "s_waitcnt lgkmcnt(0)\n" \
    "v_mfma_f32_32x32x16_bf16 v[96:111], v[140:143], v[128:131], 0\n" \
    "v_exp_f32 v117, v117\n" \
    "v_exp_f32 v118, v118\n" \
    "v_exp_f32 v119, v119\n" \
    "v_exp_f32 v120, v120\n" \
    "v_exp_f32 v121, v121\n" \
    "v_smfmac_f32_32x32x32_bf16 v[80:95], v[144:147], v[112:119], v47\n" \
    "v_exp_f32 v122, v122\n" \
    "v_exp_f32 v123, v123\n" \
    "v_exp_f32 v124, v124\n" \
    "v_exp_f32 v125, v125\n" \
    "v_exp_f32 v126, v126\n" \
    "v_exp_f32 v127, v127\n" \
    "s_setprio 0\n" \
    "v_exp_f32 v96, v96\n" \
    "v_exp_f32 v97, v97\n" \
    "v_smfmac_f32_32x32x32_bf16 v[80:95], v[148:151], v[120:127], v47\n" \
    "ds_read_b128 v[136:139], %[ka] offset:33792\n" \
    "ds_read_b128 v[144:147], %[va] offset:33792\n" \
    "ds_read_b128 v[148:151], %[va] offset:34336\n" \
    "v_exp_f32 v98, v98\n" \
    "v_exp_f32 v99, v99\n" \
    "v_exp_f32 v100, v100\n" \
    "v_mfma_f32_32x32x16_bf16 v[112:127], v[140:143], v[132:135], 0\n" \
    "v_exp_f32 v101, v101\n" \
    "v_exp_f32 v102, v102\n" \
    "v_exp_f32 v103, v103\n" \
    "v_exp_f32 v104, v104\n" \
    "v_exp_f32 v105, v105\n" \
    "v_smfmac_f32_32x32x32_bf16 v[64:79], v[152:155], v[96:103], v47\n" \
    "s_movk_i32 s60, 12288\n" \
    "buffer_load_dwordx4 v[220:223], %[hgo], %[rs], s60 offen offset:3072 sc1\n" \
    "v_exp_f32 v106, v106\n" \
    "v_exp_f32 v107, v107\n" \
    "v_exp_f32 v108, v108\n" \
    "v_exp_f32 v109, v109\n" \
    "v_exp_f32 v110, v110\n" \
    "v_exp_f32 v111, v111\n" \
    "v_exp_f32 v112, v112\n" \
    "v_exp_f32 v113, v113\n" \
    "v_smfmac_f32_32x32x32_bf16 v[64:79], v[156:159], v[104:111], v47\n" \
    "v_exp_f32 v114, v114\n" \
    "v_exp_f32 v115, v115\n" \
    "v_exp_f32 v116, v116\n" \
    "s_waitcnt lgkmcnt(0)\n" \
    "v_mfma_f32_32x32x16_bf16 v[96:111], v[136:139], v[128:131], 0\n" \
    "v_exp_f32 v117, v117\n" \
    "v_exp_f32 v118, v118\n" \
    "v_exp_f32 v119, v119\n" \
    "v_exp_f32 v120, v120\n" \
    "v_exp_f32 v121, v121\n" \
    "v_smfmac_f32_32x32x32_bf16 v[80:95], v[152:155], v[112:119], v47\n" \
    "v_exp_f32 v122, v122\n" \
    "v_exp_f32 v123, v123\n" \
    "v_exp_f32 v124, v124\n" \
    "v_exp_f32 v125, v125\n" \
    "v_exp_f32 v126, v126\n" \
    "v_exp_f32 v127, v127\n" \
    "s_barrier\n" \
    "s_setprio 3\n" \
    "v_exp_f32 v96, v96\n" \
    "v_exp_f32 v97, v97\n" \
    "v_smfmac_f32_32x32x32_bf16 v[80:95], v[156:159], v[120:127], v47\n" \
    "ds_read_b128 v[140:143], %[ka] offset:34816\n" \
    "ds_read_b128 v[152:155], %[va] offset:34880\n" \
    "ds_read_b128 v[156:159], %[va] offset:35424\n" \
    "v_exp_f32 v98, v98\n" \
    "v_exp_f32 v99, v99\n" \
    "v_exp_f32 v100, v100\n" \
    "v_mfma_f32_32x32x16_bf16 v[112:127], v[136:139], v[132:135], 0\n" \
    "s_movk_i32 s60, 4096\n" \
    "buffer_load_dwordx4 v[184:187], %[hgo], %[rs], s60 offen offset:2048 sc1\n" \
    "v_exp_f32 v101, v101\n" \
    "v_exp_f32 v102, v102\n" \
    "v_exp_f32 v103, v103\n" \
    "v_exp_f32 v104, v104\n" \
    "v_exp_f32 v105, v105\n" \
    "v_smfmac_f32_32x32x32_bf16 v[64:79], v[144:147], v[96:103], v47\n" \
    "v_exp_f32 v106, v106\n" \
    "v_exp_f32 v107, v107\n" \
    "v_exp_f32 v108, v108\n" \
    "v_exp_f32 v109, v109\n" \
    "v_exp_f32 v110, v110\n" \
    "v_exp_f32 v111, v111\n" \
    "v_exp_f32 v112, v112\n" \
    "v_exp_f32 v113, v113\n" \
    "v_smfmac_f32_32x32x32_bf16 v[64:79], v[148:151], v[104:111], v47\n" \
    "s_movk_i32 s60, 4096\n" \
    "buffer_load_dwordx4 v[188:191], %[hgo], %[rs], s60 offen offset:3072 sc1\n" \
    "v_exp_f32 v114, v114\n" \
    "v_exp_f32 v115, v115\n" \
    "v_exp_f32 v116, v116\n" \
    "s_waitcnt lgkmcnt(0)\n" \
    "v_mfma_f32_32x32x16_bf16 v[96:111], v[140:143], v[128:131], 0\n" \
    "ds_write_b128 %[kw], v[240:243] offset:16896\n" \
    "v_exp_f32 v117, v117\n" \
    "v_exp_f32 v118, v118\n" \
    "v_exp_f32 v119, v119\n" \
    "v_exp_f32 v120, v120\n" \
    "v_exp_f32 v121, v121\n" \
    "v_smfmac_f32_32x32x32_bf16 v[80:95], v[144:147], v[112:119], v47\n" \
    "v_exp_f32 v122, v122\n" \
    "v_exp_f32 v123, v123\n" \
    "v_exp_f32 v124, v124\n" \
    "v_exp_f32 v125, v125\n" \
    "v_exp_f32 v126, v126\n" \
    "v_exp_f32 v127, v127\n" \
    "s_setprio 2\n" \
    "v_exp_f32 v96, v96\n" \
    "v_exp_f32 v97, v97\n" \
    "v_smfmac_f32_32x32x32_bf16 v[80:95], v[148:151], v[120:127], v47\n" \
    "ds_read_b128 v[136:139], %[ka] offset:35840\n" \
    "ds_read_b128 v[144:147], %[va] offset:35968\n" \
    "ds_read_b128 v[148:151], %[va] offset:36512\n" \
    "v_exp_f32 v98, v98\n" \
    "v_exp_f32 v99, v99\n" \
    "v_exp_f32 v100, v100\n" \
    "v_mfma_f32_32x32x16_bf16 v[112:127], v[140:143], v[132:135], 0\n" \
    "v_exp_f32 v101, v101\n" \
    "v_exp_f32 v102, v102\n" \
    "v_exp_f32 v103, v103\n" \
    "v_exp_f32 v104, v104\n" \
    "v_exp_f32 v105, v105\n" \
    "v_smfmac_f32_32x32x32_bf16 v[64:79], v[152:155], v[96:103], v47\n" \
    "s_movk_i32 s60, 16384\n" \
    "buffer_load_dwordx4 v[224:227], %[hgo], %[rs], s60 offen offset:0 sc1\n" \
    "v_exp_f32 v106, v106\n" \
    "v_exp_f32 v107, v107\n" \
    "v_exp_f32 v108, v108\n" \
    "v_exp_f32 v109, v109\n" \
    "v_exp_f32 v110, v110\n" \
    "v_exp_f32 v111, v111\n" \
    "v_exp_f32 v112, v112\n" \
    "v_exp_f32 v113, v113\n" \
    "v_smfmac_f32_32x32x32_bf16 v[64:79], v[156:159], v[104:111], v47\n" \
    "v_exp_f32 v114, v114\n" \
    "v_exp_f32 v115, v115\n" \
    "v_exp_f32 v116, v116\n" \
    "s_waitcnt lgkmcnt(0)\n" \
    "v_mfma_f32_32x32x16_bf16 v[96:111], v[136:139], v[128:131], 0\n" \
    "ds_write_b128 %[vw], v[244:247] offset:16896\n" \
    "v_exp_f32 v117, v117\n" \
    "v_exp_f32 v118, v118\n" \
    "v_exp_f32 v119, v119\n" \
    "v_exp_f32 v120, v120\n" \
    "v_exp_f32 v121, v121\n" \
    "v_smfmac_f32_32x32x32_bf16 v[80:95], v[152:155], v[112:119], v47\n" \
    "v_exp_f32 v122, v122\n" \
    "v_exp_f32 v123, v123\n" \
    "v_exp_f32 v124, v124\n" \
    "v_exp_f32 v125, v125\n" \
    "v_exp_f32 v126, v126\n" \
    "v_exp_f32 v127, v127\n" \
    "s_setprio 1\n" \
    "v_exp_f32 v96, v96\n" \
    "v_exp_f32 v97, v97\n" \
    "v_smfmac_f32_32x32x32_bf16 v[80:95], v[156:159], v[120:127], v47\n" \
    "ds_read_b128 v[140:143], %[ka] offset:36864\n" \
    "ds_read_b128 v[152:155], %[va] offset:37056\n" \
    "ds_read_b128 v[156:159], %[va] offset:37600\n" \
    "v_exp_f32 v98, v98\n" \
    "v_exp_f32 v99, v99\n" \
    "v_exp_f32 v100, v100\n" \
    "v_mfma_f32_32x32x16_bf16 v[112:127], v[136:139], v[132:135], 0\n" \
    "s_movk_i32 s60, 16384\n" \
    "buffer_load_dwordx4 v[228:231], %[hgo], %[rs], s60 offen offset:1024 sc1\n" \
    "v_exp_f32 v101, v101\n" \
    "v_exp_f32 v102, v102\n" \
    "v_exp_f32 v103, v103\n" \
    "v_exp_f32 v104, v104\n" \
    "v_exp_f32 v105, v105\n" \
    "v_smfmac_f32_32x32x32_bf16 v[64:79], v[144:147], v[96:103], v47\n" \
    "v_exp_f32 v106, v106\n" \
    "v_exp_f32 v107, v107\n" \
    "v_exp_f32 v108, v108\n" \
    "v_exp_f32 v109, v109\n" \
    "v_exp_f32 v110, v110\n" \
    "v_exp_f32 v111, v111\n" \
    "v_exp_f32 v112, v112\n" \
    "v_exp_f32 v113, v113\n" \
    "v_smfmac_f32_32x32x32_bf16 v[64:79], v[148:151], v[104:111], v47\n" \
    "s_movk_i32 s60, 16384\n" \
    "buffer_load_dwordx4 v[232:235], %[hgo], %[rs], s60 offen offset:2048 sc1\n" \
    "v_exp_f32 v114, v114\n" \
    "v_exp_f32 v115, v115\n" \
    "v_exp_f32 v116, v116\n" \
    "s_waitcnt lgkmcnt(0)\n" \
    "v_mfma_f32_32x32x16_bf16 v[96:111], v[140:143], v[128:131], 0\n" \
    "v_exp_f32 v117, v117\n" \
    "v_exp_f32 v118, v118\n" \
    "v_exp_f32 v119, v119\n" \
    "v_exp_f32 v120, v120\n" \
    "v_exp_f32 v121, v121\n" \
    "v_smfmac_f32_32x32x32_bf16 v[80:95], v[144:147], v[112:119], v47\n" \
    "v_exp_f32 v122, v122\n" \
    "v_exp_f32 v123, v123\n" \
    "v_exp_f32 v124, v124\n" \
    "v_exp_f32 v125, v125\n" \
    "v_exp_f32 v126, v126\n" \
    "v_exp_f32 v127, v127\n" \
    "s_setprio 0\n" \
    "v_exp_f32 v96, v96\n" \
    "v_exp_f32 v97, v97\n" \
    "v_smfmac_f32_32x32x32_bf16 v[80:95], v[148:151], v[120:127], v47\n" \
    "ds_read_b128 v[136:139], %[ka] offset:0\n" \
    "ds_read_b128 v[144:147], %[va] offset:0\n" \
    "ds_read_b128 v[148:151], %[va] offset:544\n" \
    "v_exp_f32 v98, v98\n" \
    "v_exp_f32 v99, v99\n" \
    "v_exp_f32 v100, v100\n" \
    "v_mfma_f32_32x32x16_bf16 v[112:127], v[140:143], v[132:135], 0\n" \
    "v_exp_f32 v101, v101\n" \
    "v_exp_f32 v102, v102\n" \
    "v_exp_f32 v103, v103\n" \
    "v_exp_f32 v104, v104\n" \
    "v_exp_f32 v105, v105\n" \
    "v_smfmac_f32_32x32x32_bf16 v[64:79], v[152:155], v[96:103], v47\n" \
    "s_movk_i32 s60, 16384\n" \
    "buffer_load_dwordx4 v[236:239], %[hgo], %[rs], s60 offen offset:3072 sc1\n" \
    "v_exp_f32 v106, v106\n" \
    "v_exp_f32 v107, v107\n" \
    "v_exp_f32 v108, v108\n" \
    "v_exp_f32 v109, v109\n" \
    "v_exp_f32 v110, v110\n" \
    "v_exp_f32 v111, v111\n" \
    "v_exp_f32 v112, v112\n" \
    "v_exp_f32 v113, v113\n" \
    "v_smfmac_f32_32x32x32_bf16 v[64:79], v[156:159], v[104:111], v47\n" \
    "v_exp_f32 v114, v114\n" \
    "v_exp_f32 v115, v115\n" \
    "v_exp_f32 v116, v116\n" \
    "s_waitcnt lgkmcnt(0)\n" \
    "v_mfma_f32_32x32x16_bf16 v[96:111], v[136:139], v[128:131], 0\n" \
    "v_exp_f32 v117, v117\n" \
    "v_exp_f32 v118, v118\n" \
    "v_exp_f32 v119, v119\n" \
    "v_exp_f32 v120, v120\n" \
    "v_exp_f32 v121, v121\n" \
    "v_smfmac_f32_32x32x32_bf16 v[80:95], v[152:155], v[112:119], v47\n" \
    "v_exp_f32 v122, v122\n" \
    "v_exp_f32 v123, v123\n" \
    "v_exp_f32 v124, v124\n" \
    "v_exp_f32 v125, v125\n" \
    "v_exp_f32 v126, v126\n" \
    "v_exp_f32 v127, v127\n" \
    "s_barrier\n" \
    "s_setprio 3\n" \
    "v_exp_f32 v96, v96\n" \
    "v_exp_f32 v97, v97\n" \
    "v_smfmac_f32_32x32x32_bf16 v[80:95], v[156:159], v[120:127], v47\n" \
    "ds_read_b128 v[140:143], %[ka] offset:1024\n" \
    "ds_read_b128 v[152:155], %[va] offset:1088\n" \
    "ds_read_b128 v[156:159], %[va] offset:1632\n" \
    "v_exp_f32 v98, v98\n" \
    "v_exp_f32 v99, v99\n" \
    "v_exp_f32 v100, v100\n" \
    "v_mfma_f32_32x32x16_bf16 v[112:127], v[136:139], v[132:135], 0\n" \
    "s_add_u32 s60, %[w2l], 196608\n" \
    "s_addc_u32 s61, %[w2h], 0\n" \
    "s_add_u32 m0, %[wdst], 33792\n" \
    "s_nop 0\n" \
    "global_load_lds_dwordx4 %[w3off], s[60:61]\n" \
    "v_exp_f32 v101, v101\n" \
    "v_exp_f32 v102, v102\n" \
    "v_exp_f32 v103, v103\n" \
    "v_exp_f32 v104, v104\n" \
    "v_exp_f32 v105, v105\n" \
    "v_smfmac_f32_32x32x32_bf16 v[64:79], v[144:147], v[96:103], v47\n" \
    "v_exp_f32 v106, v106\n" \
    "v_exp_f32 v107, v107\n" \
    "v_exp_f32 v108, v108\n" \
    "v_exp_f32 v109, v109\n" \
    "v_exp_f32 v110, v110\n" \
    "v_exp_f32 v111, v111\n" \
    "v_exp_f32 v112, v112\n" \
    "v_exp_f32 v113, v113\n" \
    "v_smfmac_f32_32x32x32_bf16 v[64:79], v[148:151], v[104:111], v47\n" \
    "s_add_u32 s60, %[w2l], 212992\n" \
    "s_addc_u32 s61, %[w2h], 0\n" \
    "s_add_u32 m0, %[wdst], 35840\n" \
    "s_nop 0\n" \
    "global_load_lds_dwordx4 %[w3off], s[60:61]\n" \
    "v_exp_f32 v114, v114\n" \
    "v_exp_f32 v115, v115\n" \
    "v_exp_f32 v116, v116\n" \
    "s_waitcnt lgkmcnt(0)\n" \
    "v_mfma_f32_32x32x16_bf16 v[96:111], v[140:143], v[128:131], 0\n" \
    "v_exp_f32 v117, v117\n" \
    "v_exp_f32 v118, v118\n" \
    "v_exp_f32 v119, v119\n" \
    "v_exp_f32 v120, v120\n" \
    "v_exp_f32 v121, v121\n" \
    "v_smfmac_f32_32x32x32_bf16 v[80:95], v[144:147], v[112:119], v47\n" \
    "v_exp_f32 v122, v122\n" \
    "v_exp_f32 v123, v123\n" \
    "v_exp_f32 v124, v124\n" \
    "v_exp_f32 v125, v125\n" \
    "v_exp_f32 v126, v126\n" \
    "v_exp_f32 v127, v127\n" \
    "s_setprio 2\n" \
    "v_exp_f32 v96, v96\n" \
    "v_exp_f32 v97, v97\n" \
    "v_smfmac_f32_32x32x32_bf16 v[80:95], v[148:151], v[120:127], v47\n" \
    "ds_read_b128 v[136:139], %[ka] offset:2048\n" \
    "ds_read_b128 v[144:147], %[va] offset:2176\n" \
    "ds_read_b128 v[148:151], %[va] offset:2720\n" \
    "v_exp_f32 v98, v98\n" \
    "v_exp_f32 v99, v99\n" \
    "v_exp_f32 v100, v100\n" \
    "v_mfma_f32_32x32x16_bf16 v[112:127], v[140:143], v[132:135], 0\n" \
    "v_exp_f32 v101, v101\n" \
    "v_exp_f32 v102, v102\n" \
    "v_exp_f32 v103, v103\n" \
    "v_exp_f32 v104, v104\n" \
    "v_exp_f32 v105, v105\n" \
    "v_smfmac_f32_32x32x32_bf16 v[64:79], v[152:155], v[96:103], v47\n" \
    "s_add_u32 s60, %[w2l], 229376\n" \
    "s_addc_u32 s61, %[w2h], 0\n" \
    "s_add_u32 m0, %[wdst], 37888\n" \
    "s_nop 0\n" \
    "global_load_lds_dwordx4 %[w3off], s[60:61]\n" \
    "v_exp_f32 v106, v106\n" \
    "v_exp_f32 v107, v107\n" \
    "v_exp_f32 v108, v108\n" \
    "v_exp_f32 v109, v109\n" \
    "v_exp_f32 v110, v110\n" \
    "v_exp_f32 v111, v111\n" \
    "v_exp_f32 v112, v112\n" \
    "v_exp_f32 v113, v113\n" \
    "v_smfmac_f32_32x32x32_bf16 v[64:79], v[156:159], v[104:111], v47\n" \
    "v_exp_f32 v114, v114\n" \
    "v_exp_f32 v115, v115\n" \
    "v_exp_f32 v116, v116\n" \
    "s_waitcnt lgkmcnt(0)\n" \
    "v_mfma_f32_32x32x16_bf16 v[96:111], v[136:139], v[128:131], 0\n" \
    "v_exp_f32 v117, v117\n" \
    "v_exp_f32 v118, v118\n" \
    "v_exp_f32 v119, v119\n" \
    "v_exp_f32 v120, v120\n" \
    "v_exp_f32 v121, v121\n" \
    "v_smfmac_f32_32x32x32_bf16 v[80:95], v[152:155], v[112:119], v47\n" \
    "v_exp_f32 v122, v122\n" \
    "v_exp_f32 v123, v123\n" \
    "v_exp_f32 v124, v124\n" \
    "v_exp_f32 v125, v125\n" \
    "v_exp_f32 v126, v126\n" \
    "v_exp_f32 v127, v127\n" \
    "s_setprio 1\n" \
    "v_exp_f32 v96, v96\n" \
    "v_exp_f32 v97, v97\n" \
    "v_smfmac_f32_32x32x32_bf16 v[80:95], v[156:159], v[120:127], v47\n" \
    "ds_read_b128 v[140:143], %[ka] offset:3072\n" \
    "ds_read_b128 v[152:155], %[va] offset:3264\n" \
    "ds_read_b128 v[156:159], %[va] offset:3808\n" \
    "v_exp_f32 v98, v98\n" \
    "v_exp_f32 v99, v99\n" \
    "v_exp_f32 v100, v100\n" \
    "v_mfma_f32_32x32x16_bf16 v[112:127], v[136:139], v[132:135], 0\n" \
    "v_exp_f32 v101, v101\n" \
    "v_exp_f32 v102, v102\n" \
    "v_exp_f32 v103, v103\n" \
    "v_exp_f32 v104, v104\n" \
    "v_exp_f32 v105, v105\n" \
    "v_smfmac_f32_32x32x32_bf16 v[64:79], v[144:147], v[96:103], v47\n" \
    "s_movk_i32 s60, 20480\n" \
    "buffer_load_dwordx4 v[240:243], %[hgo], %[rs], s60 offen offset:0 sc1\n" \
    "v_exp_f32 v106, v106\n" \
    "v_exp_f32 v107, v107\n" \
    "v_exp_f32 v108, v108\n" \
    "v_exp_f32 v109, v109\n" \
    "v_exp_f32 v110, v110\n" \
    "v_exp_f32 v111, v111\n" \
    "v_exp_f32 v112, v112\n" \
    "v_exp_f32 v113, v113\n" \
    "v_smfmac_f32_32x32x32_bf16 v[64:79], v[148:151], v[104:111], v47\n" \
    "v_exp_f32 v114, v114\n" \
    "v_exp_f32 v115, v115\n" \
    "v_exp_f32 v116, v116\n" \
    "s_waitcnt lgkmcnt(0)\n" \
    "v_mfma_f32_32x32x16_bf16 v[96:111], v[140:143], v[128:131], 0\n" \
    "v_exp_f32 v117, v117\n" \
    "v_exp_f32 v118, v118\n" \
    "v_exp_f32 v119, v119\n" \
    "v_exp_f32 v120, v120\n" \
    "v_exp_f32 v121, v121\n" \
    "v_smfmac_f32_32x32x32_bf16 v[80:95], v[144:147], v[112:119], v47\n" \
    "v_exp_f32 v122, v122\n" \
    "v_exp_f32 v123, v123\n" \
    "v_exp_f32 v124, v124\n" \
    "v_exp_f32 v125, v125\n" \
    "v_exp_f32 v126, v126\n" \
    "v_exp_f32 v127, v127\n" \
    "s_setprio 0\n" \
    "v_exp_f32 v96, v96\n" \
    "v_exp_f32 v97, v97\n" \
    "v_smfmac_f32_32x32x32_bf16 v[80:95], v[148:151], v[120:127], v47\n" \
    "ds_read_b128 v[136:139], %[ka] offset:16896\n" \
    "ds_read_b128 v[144:147], %[va] offset:16896\n" \
    "ds_read_b128 v[148:151], %[va] offset:17440\n" \
    "v_exp_f32 v98, v98\n" \
    "v_exp_f32 v99, v99\n" \
    "v_exp_f32 v100, v100\n" \
    "v_mfma_f32_32x32x16_bf16 v[112:127], v[140:143], v[132:135], 0\n" \
    "buffer_load_dwordx4 v[244:247], %[hgo], %[rs], %[h21] offen sc1\n" \
    "v_exp_f32 v101, v101\n" \
    "v_exp_f32 v102, v102\n" \
    "v_exp_f32 v103, v103\n" \
    "v_exp_f32 v104, v104\n" \
    "v_exp_f32 v105, v105\n" \
    "v_smfmac_f32_32x32x32_bf16 v[64:79], v[152:155], v[96:103], v47\n" \
    "v_exp_f32 v106, v106\n" \
    "v_exp_f32 v107, v107\n" \
    "v_exp_f32 v108, v108\n" \
    "v_exp_f32 v109, v109\n" \
    "v_exp_f32 v110, v110\n" \
    "v_exp_f32 v111, v111\n" \
    "v_exp_f32 v112, v112\n" \
    "v_exp_f32 v113, v113\n" \
    "v_smfmac_f32_32x32x32_bf16 v[64:79], v[156:159], v[104:111], v47\n" \
    "v_exp_f32 v114, v114\n" \
    "v_exp_f32 v115, v115\n" \
    "v_exp_f32 v116, v116\n" \
    "s_waitcnt lgkmcnt(0)\n" \
    "v_mfma_f32_32x32x16_bf16 v[96:111], v[136:139], v[128:131], 0\n" \
    "v_exp_f32 v117, v117\n" \
    "v_exp_f32 v118, v118\n" \
    "v_exp_f32 v119, v119\n" \
    "v_exp_f32 v120, v120\n" \
    "v_exp_f32 v121, v121\n" \
    "v_smfmac_f32_32x32x32_bf16 v[80:95], v[152:155], v[112:119], v47\n" \
    "v_exp_f32 v122, v122\n" \
    "v_exp_f32 v123, v123\n" \
    "v_exp_f32 v124, v124\n" \
    "v_exp_f32 v125, v125\n" \
    "v_exp_f32 v126, v126\n" \
    "v_exp_f32 v127, v127\n" \
    "s_barrier\n" \
    "s_setprio 3\n" \
    "v_exp_f32 v96, v96\n" \
    "v_exp_f32 v97, v97\n" \
    "v_smfmac_f32_32x32x32_bf16 v[80:95], v[156:159], v[120:127], v47\n" \
    "ds_read_b128 v[140:143], %[ka] offset:17920\n" \
    "ds_read_b128 v[152:155], %[va] offset:17984\n" \
    "ds_read_b128 v[156:159], %[va] offset:18528\n" \
    "v_exp_f32 v98, v98\n" \
    "v_exp_f32 v99, v99\n" \
    "v_exp_f32 v100, v100\n" \
    "v_mfma_f32_32x32x16_bf16 v[112:127], v[136:139], v[132:135], 0\n" \
    "v_exp_f32 v101, v101\n" \
    "v_exp_f32 v102, v102\n" \
    "v_exp_f32 v103, v103\n" \
    "v_exp_f32 v104, v104\n" \
    "v_exp_f32 v105, v105\n" \
    "v_smfmac_f32_32x32x32_bf16 v[64:79], v[144:147], v[96:103], v47\n" \
    "v_exp_f32 v106, v106\n" \
    "v_exp_f32 v107, v107\n" \
    "v_exp_f32 v108, v108\n" \
    "v_exp_f32 v109, v109\n" \
    "v_exp_f32 v110, v110\n" \
    "v_exp_f32 v111, v111\n" \
    "v_exp_f32 v112, v112\n" \
    "v_exp_f32 v113, v113\n" \
    "v_smfmac_f32_32x32x32_bf16 v[64:79], v[148:151], v[104:111], v47\n" \
    "v_exp_f32 v114, v114\n" \
    "v_exp_f32 v115, v115\n" \
    "v_exp_f32 v116, v116\n" \
    "s_waitcnt lgkmcnt(0)\n" \
    "v_mfma_f32_32x32x16_bf16 v[96:111], v[140:143], v[128:131], 0\n" \
    "v_exp_f32 v117, v117\n" \
    "v_exp_f32 v118, v118\n" \
    "v_exp_f32 v119, v119\n" \
    "v_exp_f32 v120, v120\n" \
    "v_exp_f32 v121, v121\n" \
    "v_smfmac_f32_32x32x32_bf16 v[80:95], v[144:147], v[112:119], v47\n" \
    "v_exp_f32 v122, v122\n" \
    "v_exp_f32 v123, v123\n" \
    "v_exp_f32 v124, v124\n" \
    "v_exp_f32 v125, v125\n" \
    "v_exp_f32 v126, v126\n" \
    "v_exp_f32 v127, v127\n" \
    "s_setprio 2\n" \
    "v_exp_f32 v96, v96\n" \
    "v_exp_f32 v97, v97\n" \
    "v_smfmac_f32_32x32x32_bf16 v[80:95], v[148:151], v[120:127], v47\n" \
    "ds_read_b128 v[136:139], %[ka] offset:18944\n" \
    "ds_read_b128 v[144:147], %[va] offset:19072\n" \
    "ds_read_b128 v[148:151], %[va] offset:19616\n" \
    "v_exp_f32 v98, v98\n" \
    "v_exp_f32 v99, v99\n" \
    "v_exp_f32 v100, v100\n" \
    "v_mfma_f32_32x32x16_bf16 v[112:127], v[140:143], v[132:135], 0\n" \
    "v_exp_f32 v101, v101\n" \
    "v_exp_f32 v102, v102\n" \
    "v_exp_f32 v103, v103\n" \
    "v_exp_f32 v104, v104\n" \
    "v_exp_f32 v105, v105\n" \
    "v_smfmac_f32_32x32x32_bf16 v[64:79], v[152:155], v[96:103], v47\n" \
    "v_exp_f32 v106, v106\n" \
    "v_exp_f32 v107, v107\n" \
    "v_exp_f32 v108, v108\n" \
    "v_exp_f32 v109, v109\n" \
    "v_exp_f32 v110, v110\n" \
    "v_exp_f32 v111, v111\n" \
    "v_exp_f32 v112, v112\n" \
    "v_exp_f32 v113, v113\n" \
    "v_smfmac_f32_32x32x32_bf16 v[64:79], v[156:159], v[104:111], v47\n" \
    "v_exp_f32 v114, v114\n" \
    "v_exp_f32 v115, v115\n" \
    "v_exp_f32 v116, v116\n" \
    "s_waitcnt lgkmcnt(0)\n" \
    "v_mfma_f32_32x32x16_bf16 v[96:111], v[136:139], v[128:131], 0\n" \
    "v_exp_f32 v117, v117\n" \
    "v_exp_f32 v118, v118\n" \
    "v_exp_f32 v119, v119\n" \
    "v_exp_f32 v120, v120\n" \
    "v_exp_f32 v121, v121\n" \
    "v_smfmac_f32_32x32x32_bf16 v[80:95], v[152:155], v[112:119], v47\n" \
    "v_exp_f32 v122, v122\n" \
    "v_exp_f32 v123, v123\n" \
    "v_exp_f32 v124, v124\n" \
    "v_exp_f32 v125, v125\n" \
    "v_exp_f32 v126, v126\n" \
    "v_exp_f32 v127, v127\n" \
    "s_setprio 1\n" \
    "v_exp_f32 v96, v96\n" \
    "v_exp_f32 v97, v97\n" \
    "v_smfmac_f32_32x32x32_bf16 v[80:95], v[156:159], v[120:127], v47\n" \
    "ds_read_b128 v[140:143], %[ka] offset:19968\n" \
    "ds_read_b128 v[152:155], %[va] offset:20160\n" \
    "ds_read_b128 v[156:159], %[va] offset:20704\n" \
    "v_exp_f32 v98, v98\n" \
    "v_exp_f32 v99, v99\n" \
    "v_exp_f32 v100, v100\n" \
    "v_mfma_f32_32x32x16_bf16 v[112:127], v[136:139], v[132:135], 0\n" \
    "v_exp_f32 v101, v101\n" \
    "v_exp_f32 v102, v102\n" \
    "v_exp_f32 v103, v103\n" \
    "v_exp_f32 v104, v104\n" \
    "v_exp_f32 v105, v105\n" \
    "v_smfmac_f32_32x32x32_bf16 v[64:79], v[144:147], v[96:103], v47\n" \
    "v_exp_f32 v106, v106\n" \
    "v_exp_f32 v107, v107\n" \
    "v_exp_f32 v108, v108\n" \
    "v_exp_f32 v109, v109\n" \
    "v_exp_f32 v110, v110\n" \
    "v_exp_f32 v111, v111\n" \
    "v_exp_f32 v112, v112\n" \
    "v_exp_f32 v113, v113\n" \
    "v_smfmac_f32_32x32x32_bf16 v[64:79], v[148:151], v[104:111], v47\n" \
    "v_exp_f32 v114, v114\n" \
    "v_exp_f32 v115, v115\n" \
    "v_exp_f32 v116, v116\n" \
    "s_waitcnt lgkmcnt(0)\n" \
    "v_mfma_f32_32x32x16_bf16 v[96:111], v[140:143], v[128:131], 0\n" \
    "v_exp_f32 v117, v117\n" \
    "v_exp_f32 v118, v118\n" \
    "v_exp_f32 v119, v119\n" \
    "v_exp_f32 v120, v120\n" \
    "v_exp_f32 v121, v121\n" \
    "v_smfmac_f32_32x32x32_bf16 v[80:95], v[144:147], v[112:119], v47\n" \
    "v_exp_f32 v122, v122\n" \
    "v_exp_f32 v123, v123\n" \
    "v_exp_f32 v124, v124\n" \
    "v_exp_f32 v125, v125\n" \
    "v_exp_f32 v126, v126\n" \
    "v_exp_f32 v127, v127\n" \
    "s_setprio 0\n" \
    "v_exp_f32 v96, v96\n" \
    "v_exp_f32 v97, v97\n" \
    "v_smfmac_f32_32x32x32_bf16 v[80:95], v[148:151], v[120:127], v47\n" \
    "ds_read_b128 v[136:139], %[ka] offset:33792\n" \
    "ds_read_b128 v[144:147], %[va] offset:33792\n" \
    "ds_read_b128 v[148:151], %[va] offset:34336\n" \
    "v_exp_f32 v98, v98\n" \
    "v_exp_f32 v99, v99\n" \
    "v_exp_f32 v100, v100\n" \
    "v_mfma_f32_32x32x16_bf16 v[112:127], v[140:143], v[132:135], 0\n" \
    "v_exp_f32 v101, v101\n" \
    "v_exp_f32 v102, v102\n" \
    "v_exp_f32 v103, v103\n" \
    "v_exp_f32 v104, v104\n" \
    "v_exp_f32 v105, v105\n" \
    "v_smfmac_f32_32x32x32_bf16 v[64:79], v[152:155], v[96:103], v47\n" \
    "v_exp_f32 v106, v106\n" \
    "v_exp_f32 v107, v107\n" \
    "v_exp_f32 v108, v108\n" \
    "v_exp_f32 v109, v109\n" \
    "v_exp_f32 v110, v110\n" \
    "v_exp_f32 v111, v111\n" \
    "v_exp_f32 v112, v112\n" \
    "v_exp_f32 v113, v113\n" \
    "v_smfmac_f32_32x32x32_bf16 v[64:79], v[156:159], v[104:111], v47\n" \
    "v_exp_f32 v114, v114\n" \
    "v_exp_f32 v115, v115\n" \
    "v_exp_f32 v116, v116\n" \
    "s_waitcnt lgkmcnt(0)\n" \
    "v_mfma_f32_32x32x16_bf16 v[96:111], v[136:139], v[128:131], 0\n" \
    "v_exp_f32 v117, v117\n" \
    "v_exp_f32 v118, v118\n" \
    "v_exp_f32 v119, v119\n" \
    "v_exp_f32 v120, v120\n" \
    "v_exp_f32 v121, v121\n" \
    "v_smfmac_f32_32x32x32_bf16 v[80:95], v[152:155], v[112:119], v47\n" \
    "v_exp_f32 v122, v122\n" \
    "v_exp_f32 v123, v123\n" \
    "v_exp_f32 v124, v124\n" \
    "v_exp_f32 v125, v125\n" \
    "v_exp_f32 v126, v126\n" \
    "v_exp_f32 v127, v127\n" \
    "s_nop 1\n" \
    "v_smfmac_f32_32x32x32_bf16 v[80:95], v[156:159], v[120:127], v47\n" \
    "s_setprio 0\n" \
    "s_cmp_lt_u32 %[w], 4\n" \
    "s_cbranch_scc1 4f\n" \
    "ds_write_b128 %[mw], v[64:67]\n" \
    "ds_write_b128 %[mw], v[68:71] offset:1024\n" \
    "ds_write_b32 %[lw], v72\n" \
    "s_nop 7\n" \
    "v_mov_b32 v64, v80\n" \
    "v_mov_b32 v65, v81\n" \
    "v_mov_b32 v66, v82\n" \
    "v_mov_b32 v67, v83\n" \
    "v_mov_b32 v68, v84\n" \
    "v_mov_b32 v69, v85\n" \
    "v_mov_b32 v70, v86\n" \
    "v_mov_b32 v71, v87\n" \
    "v_mov_b32 v72, v88\n" \
    "s_branch 5f\n" \
    "4:\n" \
    "s_nop 9\n" \
    "ds_write_b128 %[mw], v[80:83]\n" \
    "ds_write_b128 %[mw], v[84:87] offset:1024\n" \
    "ds_write_b32 %[lw], v88\n" \
    "5:\n" \
    "v_xor_b32 v98, 0x2000, %[mw]\n" \
    "v_xor_b32 v99, 0x400, %[lw]\n" \
    "s_waitcnt vmcnt(0) lgkmcnt(0)\n" \
    "s_barrier\n" \
    "ds_read_b32 v96, v99\n" \
    "ds_read_b128 v[100:103], v98\n" \
    "ds_read_b128 v[104:107], v98 offset:1024\n" \
    "s_waitcnt lgkmcnt(2)\n" \
    "v_add_f32 v72, v72, v96\n" \
    "v_rcp_f32 v97, v72\n" \
    "s_waitcnt lgkmcnt(0)\n" \
    "v_add_f32 v64, v64, v100\n" \
    "v_add_f32 v65, v65, v101\n" \
    "v_add_f32 v66, v66, v102\n" \
    "v_add_f32 v67, v67, v103\n" \
    "v_add_f32 v68, v68, v104\n" \
    "v_add_f32 v69, v69, v105\n" \
    "v_add_f32 v70, v70, v106\n" \
    "v_add_f32 v71, v71, v107\n" \
    "v_mul_f32 v100, v64, v97\n" \
    "v_mul_f32 v101, v65, v97\n" \
    "v_mul_f32 v102, v66, v97\n" \
    "v_mul_f32 v103, v67, v97\n" \
    "v_mul_f32 v104, v68, v97\n" \
    "v_mul_f32 v105, v69, v97\n" \
    "v_mul_f32 v106, v70, v97\n" \
    "v_mul_f32 v107, v71, v97\n" \
    "v_cvt_pk_bf16_f32 v108, v100, v101\n" \
    "v_cvt_pk_bf16_f32 v109, v102, v103\n" \
    "v_cvt_pk_bf16_f32 v110, v104, v105\n" \
    "v_cvt_pk_bf16_f32 v111, v106, v107\n" \
    "buffer_store_dwordx4 v[108:111], %[oso], %[rs], 0 offen sc1\n" \
    "v_maximum3_f32 v96, |v64|, |v65|, |v66|\n" \
    "v_maximum3_f32 v97, |v67|, |v68|, |v69|\n" \
    "v_cmp_nle_f32 vcc, 0x1f800000, v72\n" \
    "v_maximum3_f32 v96, |v70|, |v71|, v96\n" \
    "v_mov_b32 v98, 0x7e800000\n" \
    "s_mov_b64 %[bad], vcc\n" \
    "v_cmp_nge_f32 vcc, 0x71800000, v72\n" \
    "v_maximum3_f32 v96, v96, v97, v97\n" \
    "s_nop 0\n" \
    "s_or_b64 %[bad], %[bad], vcc\n" \
    "v_cmp_nge_f32 vcc, v98, v96\n" \
    "s_nop 1\n" \
    "s_or_b64 %[bad], %[bad], vcc\n"
#define ATT_CLOB "v46", "v47", "v96", "v97", "v98", "v99", "v100", "v101", "v102", "v103", "v104", "v105", "v106", "v107", "v108", "v109", "v110", "v111", "v112", "v113", "v114", "v115", "v116", "v117", "v118", "v119", "v120", "v121", "v122", "v123", "v124", "v125", "v126", "v127", "v136", "v137", "v138", "v139", "v140", "v141", "v142", "v143", "v144", "v145", "v146", "v147", "v148", "v149", "v150", "v151", "v152", "v153", "v154", "v155", "v156", "v157", "v158", "v159"
#define P3L_ASM \
    "v_add_u32 v56, 81920, %[wa3]\n" \
    "ds_read_b64_tr_b16 v[136:137], %[wa3] offset:58880\n" \
    "ds_read_b64_tr_b16 v[138:139], %[wa3] offset:59136\n" \
    "ds_read_b64_tr_b16 v[140:141], %[wa3] offset:59392\n" \
    "ds_read_b64_tr_b16 v[142:143], %[wa3] offset:59648\n" \
    "ds_read_b64_tr_b16 v[144:145], %[wa3] offset:60928\n" \
    "ds_read_b64_tr_b16 v[146:147], %[wa3] offset:61184\n" \
    "ds_read_b64_tr_b16 v[148:149], %[wa3] offset:61440\n" \
    "ds_read_b64_tr_b16 v[150:151], %[wa3] offset:61696\n" \
    "ds_read_b64_tr_b16 v[152:153], %[wa3] offset:62976\n" \
    "ds_read_b64_tr_b16 v[154:155], %[wa3] offset:63232\n" \
    "ds_read_b64_tr_b16 v[156:157], %[wa3] offset:63488\n" \
    "ds_read_b64_tr_b16 v[158:159], %[wa3] offset:63744\n" \
    "s_waitcnt lgkmcnt(8)\n" \
    "v_mfma_f32_32x32x16_bf16 v[64:79], v[136:139], v[160:163], 0\n" \
    "v_mfma_f32_32x32x16_bf16 v[80:95], v[140:143], v[160:163], 0\n" \
    "ds_read_b64_tr_b16 v[136:137], v56 offset:1536\n" \
    "ds_read_b64_tr_b16 v[138:139], v56 offset:1792\n" \
    "ds_read_b64_tr_b16 v[140:141], v56 offset:2048\n" \
    "ds_read_b64_tr_b16 v[142:143], v56 offset:2304\n" \
    "s_waitcnt lgkmcnt(8)\n" \
    "v_mfma_f32_32x32x16_bf16 v[64:79], v[144:147], v[164:167], v[64:79]\n" \
    "v_mfma_f32_32x32x16_bf16 v[80:95], v[148:151], v[164:167], v[80:95]\n" \
    "ds_read_b64_tr_b16 v[144:145], v56 offset:3584\n" \
    "ds_read_b64_tr_b16 v[146:147], v56 offset:3840\n" \
    "ds_read_b64_tr_b16 v[148:149], v56 offset:4096\n" \
    "ds_read_b64_tr_b16 v[150:151], v56 offset:4352\n" \
    "s_waitcnt lgkmcnt(8)\n" \
    "v_mfma_f32_32x32x16_bf16 v[64:79], v[152:155], v[168:171], v[64:79]\n" \
    "v_mfma_f32_32x32x16_bf16 v[80:95], v[156:159], v[168:171], v[80:95]\n" \
    "ds_read_b64_tr_b16 v[152:153], v56 offset:5632\n" \
    "ds_read_b64_tr_b16 v[154:155], v56 offset:5888\n" \
    "ds_read_b64_tr_b16 v[156:157], v56 offset:6144\n" \
    "ds_read_b64_tr_b16 v[158:159], v56 offset:6400\n" \
    "s_waitcnt lgkmcnt(8)\n" \
    "v_mfma_f32_32x32x16_bf16 v[64:79], v[136:139], v[172:175], v[64:79]\n" \
    "v_mfma_f32_32x32x16_bf16 v[80:95], v[140:143], v[172:175], v[80:95]\n" \
    "ds_read_b64_tr_b16 v[136:137], v56 offset:26112\n" \
    "ds_read_b64_tr_b16 v[138:139], v56 offset:26368\n" \
    "ds_read_b64_tr_b16 v[140:141], v56 offset:26624\n" \
    "ds_read_b64_tr_b16 v[142:143], v56 offset:26880\n" \
    "s_waitcnt lgkmcnt(8)\n" \
    "s_waitcnt vmcnt(0)\n" \
    "s_barrier\n" \
    "s_cmp_lg_u32 %[w], 0\n" \
    "s_cbranch_scc1 6f\n" \
    "s_mov_b64 exec, 1\n" \
    "buffer_store_dword %[tag], off, %[rs], %[ofo] sc1\n" \
    "s_mov_b64 exec, -1\n" \
    "6:\n" \
    "v_mfma_f32_32x32x16_bf16 v[64:79], v[144:147], v[176:179], v[64:79]\n" \
    "v_mfma_f32_32x32x16_bf16 v[80:95], v[148:151], v[176:179], v[80:95]\n" \
    "ds_read_b64_tr_b16 v[144:145], v56 offset:28160\n" \
    "ds_read_b64_tr_b16 v[146:147], v56 offset:28416\n" \
    "ds_read_b64_tr_b16 v[148:149], v56 offset:28672\n" \
    "ds_read_b64_tr_b16 v[150:151], v56 offset:28928\n" \
    "s_waitcnt lgkmcnt(8)\n" \
    "v_mfma_f32_32x32x16_bf16 v[64:79], v[152:155], v[180:183], v[64:79]\n" \
    "v_mfma_f32_32x32x16_bf16 v[80:95], v[156:159], v[180:183], v[80:95]\n" \
    "ds_read_b64_tr_b16 v[152:153], v56 offset:30208\n" \
    "ds_read_b64_tr_b16 v[154:155], v56 offset:30464\n" \
    "ds_read_b64_tr_b16 v[156:157], v56 offset:30720\n" \
    "ds_read_b64_tr_b16 v[158:159], v56 offset:30976\n" \
    "s_waitcnt lgkmcnt(8)\n" \
    "s_barrier\n" \
    "v_mfma_f32_32x32x16_bf16 v[64:79], v[136:139], v[184:187], v[64:79]\n" \
    "v_mfma_f32_32x32x16_bf16 v[80:95], v[140:143], v[184:187], v[80:95]\n" \
    "ds_read_b64_tr_b16 v[136:137], v56 offset:50688\n" \
    "ds_read_b64_tr_b16 v[138:139], v56 offset:50944\n" \
    "ds_read_b64_tr_b16 v[140:141], v56 offset:51200\n" \
    "ds_read_b64_tr_b16 v[142:143], v56 offset:51456\n" \
    "s_waitcnt lgkmcnt(8)\n" \
    "v_mfma_f32_32x32x16_bf16 v[64:79], v[144:147], v[188:191], v[64:79]\n" \
    "v_mfma_f32_32x32x16_bf16 v[80:95], v[148:151], v[188:191], v[80:95]\n" \
    "ds_read_b64_tr_b16 v[144:145], v56 offset:52736\n" \
    "ds_read_b64_tr_b16 v[146:147], v56 offset:52992\n" \
    "ds_read_b64_tr_b16 v[148:149], v56 offset:53248\n" \
    "ds_read_b64_tr_b16 v[150:151], v56 offset:53504\n" \
    "s_waitcnt lgkmcnt(8)\n" \
    "v_mfma_f32_32x32x16_bf16 v[64:79], v[152:155], v[192:195], v[64:79]\n" \
    "v_mfma_f32_32x32x16_bf16 v[80:95], v[156:159], v[192:195], v[80:95]\n" \
    "ds_read_b64_tr_b16 v[152:153], v56 offset:54784\n" \
    "ds_read_b64_tr_b16 v[154:155], v56 offset:55040\n" \
    "ds_read_b64_tr_b16 v[156:157], v56 offset:55296\n" \
    "ds_read_b64_tr_b16 v[158:159], v56 offset:55552\n" \
    "s_waitcnt lgkmcnt(8)\n" \
    "v_mfma_f32_32x32x16_bf16 v[64:79], v[136:139], v[196:199], v[64:79]\n" \
    "s_lshr_b32 s60, %[w], 1\n" \
    "s_mul_i32 s60, s60, 6144\n" \
    "v_add_u32 v57, s60, %[rw]\n" \
    "v_mfma_f32_32x32x16_bf16 v[80:95], v[140:143], v[196:199], v[80:95]\n" \
    "ds_read_b64_tr_b16 v[136:137], %[wa3] offset:33792\n" \
    "ds_read_b64_tr_b16 v[138:139], %[wa3] offset:34048\n" \
    "ds_read_b64_tr_b16 v[140:141], %[wa3] offset:34304\n" \
    "ds_read_b64_tr_b16 v[142:143], %[wa3] offset:34560\n" \
    "s_waitcnt lgkmcnt(8)\n" \
    "v_mfma_f32_32x32x16_bf16 v[64:79], v[144:147], v[200:203], v[64:79]\n" \
    "v_lshlrev_b32 v128, 16, %[xr0]\n" \
    "v_lshlrev_b32 v130, 16, %[xr1]\n" \
    "v_lshlrev_b32 v132, 16, %[xr2]\n" \
    "v_lshlrev_b32 v134, 16, %[xr3]\n" \
    "v_and_b32 v129, 0xffff0000, %[xr0]\n" \
    "v_and_b32 v131, 0xffff0000, %[xr1]\n" \
    "v_and_b32 v133, 0xffff0000, %[xr2]\n" \
    "v_and_b32 v135, 0xffff0000, %[xr3]\n" \
    "v_mfma_f32_32x32x16_bf16 v[80:95], v[148:151], v[200:203], v[80:95]\n" \
    "buffer_load_dword v46, %[oflo], %[rs], 0 offen sc1\n" \
    "ds_read_b64_tr_b16 v[144:145], %[wa3] offset:35840\n" \
    "ds_read_b64_tr_b16 v[146:147], %[wa3] offset:36096\n" \
    "ds_read_b64_tr_b16 v[148:149], %[wa3] offset:36352\n" \
    "ds_read_b64_tr_b16 v[150:151], %[wa3] offset:36608\n" \
    "s_waitcnt lgkmcnt(8)\n" \
    "v_mfma_f32_32x32x16_bf16 v[64:79], v[152:155], v[204:207], v[64:79]\n" \
    "v_lshlrev_b32 v48, 16, %[bq0]\n" \
    "v_lshlrev_b32 v50, 16, %[bq1]\n" \
    "v_lshlrev_b32 v52, 16, %[bq2]\n" \
    "v_lshlrev_b32 v54, 16, %[bq3]\n" \
    "v_and_b32 v49, 0xffff0000, %[bq0]\n" \
    "v_and_b32 v51, 0xffff0000, %[bq1]\n" \
    "v_and_b32 v53, 0xffff0000, %[bq2]\n" \
    "v_and_b32 v55, 0xffff0000, %[bq3]\n" \
    "v_mfma_f32_32x32x16_bf16 v[80:95], v[156:159], v[204:207], v[80:95]\n" \
    "ds_read_b64_tr_b16 v[152:153], %[wa3] offset:37888\n" \
    "ds_read_b64_tr_b16 v[154:155], %[wa3] offset:38144\n" \
    "ds_read_b64_tr_b16 v[156:157], %[wa3] offset:38400\n" \
    "ds_read_b64_tr_b16 v[158:159], %[wa3] offset:38656\n" \
    "s_waitcnt lgkmcnt(8)\n" \
    "v_mfma_f32_32x32x16_bf16 v[64:79], v[136:139], v[208:211], v[64:79]\n" \
    "v_add_f32 v128, v128, v48\n" \
    "v_add_f32 v129, v129, v49\n" \
    "v_add_f32 v130, v130, v50\n" \
    "v_add_f32 v131, v131, v51\n" \
    "v_add_f32 v132, v132, v52\n" \
    "v_add_f32 v133, v133, v53\n" \
    "v_add_f32 v134, v134, v54\n" \
    "v_add_f32 v135, v135, v55\n" \
    "v_mfma_f32_32x32x16_bf16 v[80:95], v[140:143], v[208:211], v[80:95]\n" \
    "s_barrier\n" \
    "ds_read_b64_tr_b16 v[136:137], %[wa3] offset:0\n" \
    "ds_read_b64_tr_b16 v[138:139], %[wa3] offset:256\n" \
    "ds_read_b64_tr_b16 v[140:141], %[wa3] offset:512\n" \
    "ds_read_b64_tr_b16 v[142:143], %[wa3] offset:768\n" \
    "s_waitcnt lgkmcnt(8)\n" \
    "v_mfma_f32_32x32x16_bf16 v[64:79], v[144:147], v[212:215], v[64:79]\n" \
    "v_mfma_f32_32x32x16_bf16 v[80:95], v[148:151], v[212:215], v[80:95]\n" \
    "ds_read_b64_tr_b16 v[144:145], %[wa3] offset:2048\n" \
    "ds_read_b64_tr_b16 v[146:147], %[wa3] offset:2304\n" \
    "ds_read_b64_tr_b16 v[148:149], %[wa3] offset:2560\n" \
    "ds_read_b64_tr_b16 v[150:151], %[wa3] offset:2816\n" \
    "s_waitcnt lgkmcnt(8)\n" \
    "v_mfma_f32_32x32x16_bf16 v[64:79], v[152:155], v[216:219], v[64:79]\n" \
    "v_mfma_f32_32x32x16_bf16 v[80:95], v[156:159], v[216:219], v[80:95]\n" \
    "s_waitcnt vmcnt(0)\n" \
    "s_mov_b32 s61, 0x40000\n" \
    "4:\n" \
    "v_cmp_ne_u32 vcc, v46, %[tag]\n" \
    "s_nop 1\n" \
    "s_cmp_eq_u64 vcc, 0\n" \
    "s_cbranch_scc1 5f\n" \
    "s_sleep 1\n" \
    "buffer_load_dword v46, %[oflo], %[rs], 0 offen sc1\n" \
    "s_sub_u32 s61, s61, 1\n" \
    "s_waitcnt vmcnt(0)\n" \
    "s_cbranch_scc0 4b\n" \
    "5:\n" \
    "s_movk_i32 s60, 4096\n" \
    "buffer_load_dwordx4 v[96:99], %[ogo], %[rs], 0 offen offset:0 sc1\n" \
    "buffer_load_dwordx4 v[100:103], %[ogo], %[rs], 0 offen offset:1024 sc1\n" \
    "buffer_load_dwordx4 v[104:107], %[ogo], %[rs], 0 offen offset:2048 sc1\n" \
    "buffer_load_dwordx4 v[108:111], %[ogo], %[rs], 0 offen offset:3072 sc1\n" \
    "buffer_load_dwordx4 v[112:115], %[ogo], %[rs], s60 offen offset:0 sc1\n" \
    "buffer_load_dwordx4 v[116:119], %[ogo], %[rs], s60 offen offset:1024 sc1\n" \
    "buffer_load_dwordx4 v[120:123], %[ogo], %[rs], s60 offen offset:2048 sc1\n" \
    "buffer_load_dwordx4 v[124:127], %[ogo], %[rs], s60 offen offset:3072 sc1\n" \
    "ds_read_b64_tr_b16 v[152:153], %[wa3] offset:4096\n" \
    "ds_read_b64_tr_b16 v[154:155], %[wa3] offset:4352\n" \
    "ds_read_b64_tr_b16 v[156:157], %[wa3] offset:4608\n" \
    "ds_read_b64_tr_b16 v[158:159], %[wa3] offset:4864\n" \
    "s_waitcnt lgkmcnt(8)\n" \
    "v_mfma_f32_32x32x16_bf16 v[64:79], v[136:139], v[220:223], v[64:79]\n" \
    "v_mfma_f32_32x32x16_bf16 v[80:95], v[140:143], v[220:223], v[80:95]\n" \
    "ds_read_b64_tr_b16 v[136:137], %[wa3] offset:58880\n" \
    "ds_read_b64_tr_b16 v[138:139], %[wa3] offset:59136\n" \
    "ds_read_b64_tr_b16 v[140:141], %[wa3] offset:59392\n" \
    "ds_read_b64_tr_b16 v[142:143], %[wa3] offset:59648\n" \
    "s_waitcnt lgkmcnt(8)\n" \
    "v_mfma_f32_32x32x16_bf16 v[64:79], v[144:147], v[224:227], v[64:79]\n" \
    "v_mfma_f32_32x32x16_bf16 v[80:95], v[148:151], v[224:227], v[80:95]\n" \
    "ds_read_b64_tr_b16 v[144:145], %[wa3] offset:60928\n" \
    "ds_read_b64_tr_b16 v[146:147], %[wa3] offset:61184\n" \
    "ds_read_b64_tr_b16 v[148:149], %[wa3] offset:61440\n" \
    "ds_read_b64_tr_b16 v[150:151], %[wa3] offset:61696\n" \
    "s_waitcnt lgkmcnt(8)\n" \
    "v_mfma_f32_32x32x16_bf16 v[64:79], v[152:155], v[228:231], v[64:79]\n" \
    "v_mfma_f32_32x32x16_bf16 v[80:95], v[156:159], v[228:231], v[80:95]\n" \
    "ds_read_b64_tr_b16 v[152:153], %[wa3] offset:62976\n" \
    "ds_read_b64_tr_b16 v[154:155], %[wa3] offset:63232\n" \
    "ds_read_b64_tr_b16 v[156:157], %[wa3] offset:63488\n" \
    "ds_read_b64_tr_b16 v[158:159], %[wa3] offset:63744\n" \
    "s_waitcnt lgkmcnt(8)\n" \
    "v_mfma_f32_32x32x16_bf16 v[64:79], v[136:139], v[232:235], v[64:79]\n" \
    "v_mfma_f32_32x32x16_bf16 v[80:95], v[140:143], v[232:235], v[80:95]\n" \
    "s_barrier\n" \
    "ds_read_b64_tr_b16 v[136:137], v56 offset:1536\n" \
    "ds_read_b64_tr_b16 v[138:139], v56 offset:1792\n" \
    "ds_read_b64_tr_b16 v[140:141], v56 offset:2048\n" \
    "ds_read_b64_tr_b16 v[142:143], v56 offset:2304\n" \
    "s_waitcnt lgkmcnt(8)\n" \
    "v_mfma_f32_32x32x16_bf16 v[64:79], v[144:147], v[236:239], v[64:79]\n" \
    "v_mfma_f32_32x32x16_bf16 v[80:95], v[148:151], v[236:239], v[80:95]\n" \
    "ds_read_b64_tr_b16 v[144:145], v56 offset:3584\n" \
    "ds_read_b64_tr_b16 v[146:147], v56 offset:3840\n" \
    "ds_read_b64_tr_b16 v[148:149], v56 offset:4096\n" \
    "ds_read_b64_tr_b16 v[150:151], v56 offset:4352\n" \
    "s_waitcnt lgkmcnt(8)\n" \
    "v_mfma_f32_32x32x16_bf16 v[64:79], v[152:155], v[240:243], v[64:79]\n" \
    "v_mfma_f32_32x32x16_bf16 v[80:95], v[156:159], v[240:243], v[80:95]\n" \
    "ds_read_b64_tr_b16 v[152:153], v56 offset:5632\n" \
    "ds_read_b64_tr_b16 v[154:155], v56 offset:5888\n" \
    "ds_read_b64_tr_b16 v[156:157], v56 offset:6144\n" \
    "ds_read_b64_tr_b16 v[158:159], v56 offset:6400\n" \
    "s_waitcnt lgkmcnt(8)\n" \
    "v_mfma_f32_32x32x16_bf16 v[64:79], v[136:139], v[244:247], v[64:79]\n" \
    "v_mfma_f32_32x32x16_bf16 v[80:95], v[140:143], v[244:247], v[80:95]\n" \
    "s_barrier\n" \
    "ds_read_b64_tr_b16 v[136:137], v56 offset:26112\n" \
    "ds_read_b64_tr_b16 v[138:139], v56 offset:26368\n" \
    "ds_read_b64_tr_b16 v[140:141], v56 offset:26624\n" \
    "ds_read_b64_tr_b16 v[142:143], v56 offset:26880\n" \
    "s_waitcnt lgkmcnt(8)\n" \
    "s_waitcnt vmcnt(7)\n" \
    "v_mfma_f32_32x32x16_bf16 v[64:79], v[144:147], v[96:99], v[64:79]\n" \
    "v_mfma_f32_32x32x16_bf16 v[80:95], v[148:151], v[96:99], v[80:95]\n" \
    "ds_read_b64_tr_b16 v[144:145], v56 offset:28160\n" \
    "ds_read_b64_tr_b16 v[146:147], v56 offset:28416\n" \
    "ds_read_b64_tr_b16 v[148:149], v56 offset:28672\n" \
    "ds_read_b64_tr_b16 v[150:151], v56 offset:28928\n" \
    "s_waitcnt lgkmcnt(8)\n" \
    "s_waitcnt vmcnt(6)\n" \
    "v_mfma_f32_32x32x16_bf16 v[64:79], v[152:155], v[100:103], v[64:79]\n" \
    "v_mfma_f32_32x32x16_bf16 v[80:95], v[156:159], v[100:103], v[80:95]\n" \
    "ds_read_b64_tr_b16 v[152:153], v56 offset:30208\n" \
    "ds_read_b64_tr_b16 v[154:155], v56 offset:30464\n" \
    "ds_read_b64_tr_b16 v[156:157], v56 offset:30720\n" \
    "ds_read_b64_tr_b16 v[158:159], v56 offset:30976\n" \
    "s_waitcnt lgkmcnt(8)\n" \
    "s_waitcnt vmcnt(5)\n" \
    "v_mfma_f32_32x32x16_bf16 v[64:79], v[136:139], v[104:107], v[64:79]\n" \
    "v_mfma_f32_32x32x16_bf16 v[80:95], v[140:143], v[104:107], v[80:95]\n" \
    "s_barrier\n" \
    "ds_read_b64_tr_b16 v[136:137], v56 offset:50688\n" \
    "ds_read_b64_tr_b16 v[138:139], v56 offset:50944\n" \
    "ds_read_b64_tr_b16 v[140:141], v56 offset:51200\n" \
    "ds_read_b64_tr_b16 v[142:143], v56 offset:51456\n" \
    "s_waitcnt lgkmcnt(8)\n" \
    "s_waitcnt vmcnt(4)\n" \
    "v_mfma_f32_32x32x16_bf16 v[64:79], v[144:147], v[108:111], v[64:79]\n" \
    "v_mfma_f32_32x32x16_bf16 v[80:95], v[148:151], v[108:111], v[80:95]\n" \
    "ds_read_b64_tr_b16 v[144:145], v56 offset:52736\n" \
    "ds_read_b64_tr_b16 v[146:147], v56 offset:52992\n" \
    "ds_read_b64_tr_b16 v[148:149], v56 offset:53248\n" \
    "ds_read_b64_tr_b16 v[150:151], v56 offset:53504\n" \
    "s_waitcnt lgkmcnt(8)\n" \
    "s_waitcnt vmcnt(3)\n" \
    "v_mfma_f32_32x32x16_bf16 v[64:79], v[152:155], v[112:115], v[64:79]\n" \
    "v_mfma_f32_32x32x16_bf16 v[80:95], v[156:159], v[112:115], v[80:95]\n" \
    "ds_read_b64_tr_b16 v[152:153], v56 offset:54784\n" \
    "ds_read_b64_tr_b16 v[154:155], v56 offset:55040\n" \
    "ds_read_b64_tr_b16 v[156:157], v56 offset:55296\n" \
    "ds_read_b64_tr_b16 v[158:159], v56 offset:55552\n" \
    "s_waitcnt lgkmcnt(8)\n" \
    "s_waitcnt vmcnt(2)\n" \
    "v_mfma_f32_32x32x16_bf16 v[64:79], v[136:139], v[116:119], v[64:79]\n" \
    "v_mfma_f32_32x32x16_bf16 v[80:95], v[140:143], v[116:119], v[80:95]\n" \
    "s_waitcnt lgkmcnt(4)\n" \
    "s_waitcnt vmcnt(1)\n" \
    "v_mfma_f32_32x32x16_bf16 v[64:79], v[144:147], v[120:123], v[64:79]\n" \
    "v_mfma_f32_32x32x16_bf16 v[80:95], v[148:151], v[120:123], v[80:95]\n" \
    "s_waitcnt lgkmcnt(0)\n" \
    "s_waitcnt vmcnt(0)\n" \
    "v_mfma_f32_32x32x16_bf16 v[64:79], v[152:155], v[124:127], v[64:79]\n" \
    "v_mfma_f32_32x32x16_bf16 v[80:95], v[156:159], v[124:127], v[80:95]\n" \
    "s_bitcmp1_b32 %[w], 1\n" \
    "s_barrier\n" \
    "s_nop 7\n" \
    "s_cbranch_scc1 8f\n" \
    "ds_write_b128 %[rw], v[72:75] offset:8192\n" \
    "ds_write_b128 %[rw], v[76:79] offset:9216\n" \
    "ds_write_b128 %[rw], v[80:83] offset:16384\n" \
    "ds_write_b128 %[rw], v[84:87] offset:17408\n" \
    "ds_write_b128 %[rw], v[88:91] offset:24576\n" \
    "ds_write_b128 %[rw], v[92:95] offset:25600\n" \
    "v_add_f32 v128, v128, v64\n" \
    "v_add_f32 v129, v129, v65\n" \
    "v_add_f32 v130, v130, v66\n" \
    "v_add_f32 v131, v131, v67\n" \
    "v_add_f32 v132, v132, v68\n" \
    "v_add_f32 v133, v133, v69\n" \
    "v_add_f32 v134, v134, v70\n" \
    "v_add_f32 v135, v135, v71\n" \
    "s_waitcnt lgkmcnt(0)\n" \
    "s_barrier\n" \
    "ds_read_b128 v[96:99], v57 offset:2048\n" \
    "ds_read_b128 v[100:103], v57 offset:3072\n" \
    "ds_read_b128 v[104:107], v57 offset:4096\n" \
    "ds_read_b128 v[108:111], v57 offset:5120\n" \
    "ds_read_b128 v[112:115], v57 offset:6144\n" \
    "ds_read_b128 v[116:119], v57 offset:7168\n" \
    "s_branch 9f\n" \
    "8:\n" \
    "ds_write_b128 %[rw], v[64:67] offset:0\n" \
    "ds_write_b128 %[rw], v[68:71] offset:1024\n" \
    "ds_write_b128 %[rw], v[80:83] offset:16384\n" \
    "ds_write_b128 %[rw], v[84:87] offset:17408\n" \
    "ds_write_b128 %[rw], v[88:91] offset:24576\n" \
    "ds_write_b128 %[rw], v[92:95] offset:25600\n" \
    "v_add_f32 v128, v128, v72\n" \
    "v_add_f32 v129, v129, v73\n" \
    "v_add_f32 v130, v130, v74\n" \
    "v_add_f32 v131, v131, v75\n" \
    "v_add_f32 v132, v132, v76\n" \
    "v_add_f32 v133, v133, v77\n" \
    "v_add_f32 v134, v134, v78\n" \
    "v_add_f32 v135, v135, v79\n" \
    "s_waitcnt lgkmcnt(0)\n" \
    "s_barrier\n" \
    "ds_read_b128 v[96:99], v57 offset:0\n" \
    "ds_read_b128 v[100:103], v57 offset:1024\n" \
    "ds_read_b128 v[104:107], v57 offset:4096\n" \
    "ds_read_b128 v[108:111], v57 offset:5120\n" \
    "ds_read_b128 v[112:115], v57 offset:6144\n" \
    "ds_read_b128 v[116:119], v57 offset:7168\n" \
    "9:\n" \
    "s_waitcnt lgkmcnt(4)\n" \
    "v_add_f32 v128, v128, v96\n" \
    "v_add_f32 v129, v129, v97\n" \
    "v_add_f32 v130, v130, v98\n" \
    "v_add_f32 v131, v131, v99\n" \
    "v_add_f32 v132, v132, v100\n" \
    "v_add_f32 v133, v133, v101\n" \
    "v_add_f32 v134, v134, v102\n" \
    "v_add_f32 v135, v135, v103\n" \
    "s_waitcnt lgkmcnt(0)\n" \
    "v_add_f32 v104, v104, v112\n" \
    "v_add_f32 v105, v105, v113\n" \
    "v_add_f32 v106, v106, v114\n" \
    "v_add_f32 v107, v107, v115\n" \
    "v_add_f32 v108, v108, v116\n" \
    "v_add_f32 v109, v109, v117\n" \
    "v_add_f32 v110, v110, v118\n" \
    "v_add_f32 v111, v111, v119\n" \
    "v_add_f32 v96, v128, v104\n" \
    "v_add_f32 v97, v129, v105\n" \
    "v_add_f32 v98, v130, v106\n" \
    "v_add_f32 v99, v131, v107\n" \
    "v_add_f32 v100, v132, v108\n" \
    "v_add_f32 v101, v133, v109\n" \
    "v_add_f32 v102, v134, v110\n" \
    "v_add_f32 v103, v135, v111\n" \
    "v_cvt_pk_bf16_f32 v104, v96, v97\n" \
    "v_cvt_pk_bf16_f32 v105, v98, v99\n" \
    "v_cvt_pk_bf16_f32 v106, v100, v101\n" \
    "v_cvt_pk_bf16_f32 v107, v102, v103\n" \
    "s_nop 1\n" \
    "v_permlane32_swap_b32 v104, v106\n" \
    "v_permlane32_swap_b32 v105, v107\n" \
    "s_nop 0\n" \
    "global_store_dwordx4 %[oo], v[104:107], %[out]\n" \
    ".Lend_a:\n"
#define P3H_ASM \
    "v_add_u32 v56, 81920, %[wa3]\n" \
    "ds_read_b64_tr_b16 v[136:137], %[wa3] offset:58880\n" \
    "ds_read_b64_tr_b16 v[138:139], %[wa3] offset:59136\n" \
    "ds_read_b64_tr_b16 v[140:141], %[wa3] offset:59392\n" \
    "ds_read_b64_tr_b16 v[142:143], %[wa3] offset:59648\n" \
    "ds_read_b64_tr_b16 v[144:145], %[wa3] offset:60928\n" \
    "ds_read_b64_tr_b16 v[146:147], %[wa3] offset:61184\n" \
    "ds_read_b64_tr_b16 v[148:149], %[wa3] offset:61440\n" \
    "ds_read_b64_tr_b16 v[150:151], %[wa3] offset:61696\n" \
    "ds_read_b64_tr_b16 v[152:153], %[wa3] offset:62976\n" \
    "ds_read_b64_tr_b16 v[154:155], %[wa3] offset:63232\n" \
    "ds_read_b64_tr_b16 v[156:157], %[wa3] offset:63488\n" \
    "ds_read_b64_tr_b16 v[158:159], %[wa3] offset:63744\n" \
    "s_waitcnt lgkmcnt(8)\n" \
    "v_mfma_f32_32x32x16_bf16 v[64:79], v[136:139], v[160:163], 0\n" \
    "v_mfma_f32_32x32x16_bf16 v[80:95], v[140:143], v[160:163], 0\n" \
    "ds_read_b64_tr_b16 v[136:137], v56 offset:1536\n" \
    "ds_read_b64_tr_b16 v[138:139], v56 offset:1792\n" \
    "ds_read_b64_tr_b16 v[140:141], v56 offset:2048\n" \
    "ds_read_b64_tr_b16 v[142:143], v56 offset:2304\n" \
    "s_waitcnt lgkmcnt(8)\n" \
    "v_mfma_f32_32x32x16_bf16 v[64:79], v[144:147], v[164:167], v[64:79]\n" \
    "v_mfma_f32_32x32x16_bf16 v[80:95], v[148:151], v[164:167], v[80:95]\n" \
    "ds_read_b64_tr_b16 v[144:145], v56 offset:3584\n" \
    "ds_read_b64_tr_b16 v[146:147], v56 offset:3840\n" \
    "ds_read_b64_tr_b16 v[148:149], v56 offset:4096\n" \
    "ds_read_b64_tr_b16 v[150:151], v56 offset:4352\n" \
    "s_waitcnt lgkmcnt(8)\n" \
    "v_mfma_f32_32x32x16_bf16 v[64:79], v[152:155], v[168:171], v[64:79]\n" \
    "v_mfma_f32_32x32x16_bf16 v[80:95], v[156:159], v[168:171], v[80:95]\n" \
    "ds_read_b64_tr_b16 v[152:153], v56 offset:5632\n" \
    "ds_read_b64_tr_b16 v[154:155], v56 offset:5888\n" \
    "ds_read_b64_tr_b16 v[156:157], v56 offset:6144\n" \
    "ds_read_b64_tr_b16 v[158:159], v56 offset:6400\n" \
    "s_waitcnt lgkmcnt(8)\n" \
    "v_mfma_f32_32x32x16_bf16 v[64:79], v[136:139], v[172:175], v[64:79]\n" \
    "v_mfma_f32_32x32x16_bf16 v[80:95], v[140:143], v[172:175], v[80:95]\n" \
    "ds_read_b64_tr_b16 v[136:137], v56 offset:26112\n" \
    "ds_read_b64_tr_b16 v[138:139], v56 offset:26368\n" \
    "ds_read_b64_tr_b16 v[140:141], v56 offset:26624\n" \
    "ds_read_b64_tr_b16 v[142:143], v56 offset:26880\n" \
    "s_waitcnt lgkmcnt(8)\n" \
    "s_waitcnt vmcnt(0)\n" \
    "s_barrier\n" \
    "v_mfma_f32_32x32x16_bf16 v[64:79], v[144:147], v[176:179], v[64:79]\n" \
    "s_add_u32 s60, %[w2l], 245760\n" \
    "s_addc_u32 s61, %[w2h], 0\n" \
    "s_add_u32 m0, %[wdst], 0\n" \
    "s_nop 0\n" \
    "global_load_lds_dwordx4 %[w3off], s[60:61]\n" \
    "s_add_u32 s60, %[w2lx], 245760\n" \
    "s_addc_u32 s61, %[w2hx], 0\n" \
    "s_add_u32 m0, %[wdst], -12288\n" \
    "s_nop 0\n" \
    "global_load_lds_dwordx4 %[w3off], s[60:61]\n" \
    "v_mfma_f32_32x32x16_bf16 v[80:95], v[148:151], v[176:179], v[80:95]\n" \
    "s_add_u32 s60, %[w2l], 294912\n" \
    "s_addc_u32 s61, %[w2h], 0\n" \
    "s_add_u32 m0, %[wdst], 58880\n" \
    "s_nop 0\n" \
    "global_load_lds_dwordx4 %[w3off], s[60:61]\n" \
    "s_add_u32 s60, %[w2lx], 294912\n" \
    "s_addc_u32 s61, %[w2hx], 0\n" \
    "s_add_u32 m0, %[wdst], 46592\n" \
    "s_nop 0\n" \
    "global_load_lds_dwordx4 %[w3off], s[60:61]\n" \
    "ds_read_b64_tr_b16 v[144:145], v56 offset:28160\n" \
    "ds_read_b64_tr_b16 v[146:147], v56 offset:28416\n" \
    "ds_read_b64_tr_b16 v[148:149], v56 offset:28672\n" \
    "ds_read_b64_tr_b16 v[150:151], v56 offset:28928\n" \
    "s_waitcnt lgkmcnt(8)\n" \
    "v_mfma_f32_32x32x16_bf16 v[64:79], v[152:155], v[180:183], v[64:79]\n" \
    "s_add_u32 s60, %[w2l], 262144\n" \
    "s_addc_u32 s61, %[w2h], 0\n" \
    "s_add_u32 m0, %[wdst], 2048\n" \
    "s_nop 0\n" \
    "global_load_lds_dwordx4 %[w3off], s[60:61]\n" \
    "s_add_u32 s60, %[w2lx], 262144\n" \
    "s_addc_u32 s61, %[w2hx], 0\n" \
    "s_add_u32 m0, %[wdst], -10240\n" \
    "s_nop 0\n" \
    "global_load_lds_dwordx4 %[w3off], s[60:61]\n" \
    "v_mfma_f32_32x32x16_bf16 v[80:95], v[156:159], v[180:183], v[80:95]\n" \
    "s_add_u32 s60, %[w2l], 311296\n" \
    "s_addc_u32 s61, %[w2h], 0\n" \
    "s_add_u32 m0, %[wdst], 60928\n" \
    "s_nop 0\n" \
    "global_load_lds_dwordx4 %[w3off], s[60:61]\n" \
    "s_add_u32 s60, %[w2lx], 311296\n" \
    "s_addc_u32 s61, %[w2hx], 0\n" \
    "s_add_u32 m0, %[wdst], 48640\n" \
    "s_nop 0\n" \
    "global_load_lds_dwordx4 %[w3off], s[60:61]\n" \
    "ds_read_b64_tr_b16 v[152:153], v56 offset:30208\n" \
    "ds_read_b64_tr_b16 v[154:155], v56 offset:30464\n" \
    "ds_read_b64_tr_b16 v[156:157], v56 offset:30720\n" \
    "ds_read_b64_tr_b16 v[158:159], v56 offset:30976\n" \
    "s_waitcnt lgkmcnt(8)\n" \
    "s_barrier\n" \
    "v_mfma_f32_32x32x16_bf16 v[64:79], v[136:139], v[184:187], v[64:79]\n" \
    "s_add_u32 s60, %[w2l], 278528\n" \
    "s_addc_u32 s61, %[w2h], 0\n" \
    "s_add_u32 m0, %[wdst], 4096\n" \
    "s_nop 0\n" \
    "global_load_lds_dwordx4 %[w3off], s[60:61]\n" \
    "s_add_u32 s60, %[w2lx], 278528\n" \
    "s_addc_u32 s61, %[w2hx], 0\n" \
    "s_add_u32 m0, %[wdst], -8192\n" \
    "s_nop 0\n" \
    "global_load_lds_dwordx4 %[w3off], s[60:61]\n" \
    "v_mfma_f32_32x32x16_bf16 v[80:95], v[140:143], v[184:187], v[80:95]\n" \
    "s_add_u32 s60, %[w2l], 327680\n" \
    "s_addc_u32 s61, %[w2h], 0\n" \
    "s_add_u32 m0, %[wdst], 62976\n" \
    "s_nop 0\n" \
    "global_load_lds_dwordx4 %[w3off], s[60:61]\n" \
    "s_add_u32 s60, %[w2lx], 327680\n" \
    "s_addc_u32 s61, %[w2hx], 0\n" \
    "s_add_u32 m0, %[wdst], 50688\n" \
    "s_nop 0\n" \
    "global_load_lds_dwordx4 %[w3off], s[60:61]\n" \
    "ds_read_b64_tr_b16 v[136:137], v56 offset:50688\n" \
    "ds_read_b64_tr_b16 v[138:139], v56 offset:50944\n" \
    "ds_read_b64_tr_b16 v[140:141], v56 offset:51200\n" \
    "ds_read_b64_tr_b16 v[142:143], v56 offset:51456\n" \
    "s_waitcnt lgkmcnt(8)\n" \
    "v_mfma_f32_32x32x16_bf16 v[64:79], v[144:147], v[188:191], v[64:79]\n" \
    "s_add_u32 s60, %[w2l], 327680\n" \
    "s_addc_u32 s61, %[w2h], 0\n" \
    "s_add_u32 m0, %[wdst], 83456\n" \
    "s_nop 0\n" \
    "global_load_lds_dwordx4 %[w3off], s[60:61]\n" \
    "s_add_u32 s60, %[w2lx], 344064\n" \
    "s_addc_u32 s61, %[w2hx], 0\n" \
    "s_add_u32 m0, %[wdst], 71168\n" \
    "s_nop 0\n" \
    "global_load_lds_dwordx4 %[w3off], s[60:61]\n" \
    "v_mfma_f32_32x32x16_bf16 v[80:95], v[148:151], v[188:191], v[80:95]\n" \
    "ds_read_b64_tr_b16 v[144:145], v56 offset:52736\n" \
    "ds_read_b64_tr_b16 v[146:147], v56 offset:52992\n" \
    "ds_read_b64_tr_b16 v[148:149], v56 offset:53248\n" \
    "ds_read_b64_tr_b16 v[150:151], v56 offset:53504\n" \
    "s_waitcnt lgkmcnt(8)\n" \
    "v_mfma_f32_32x32x16_bf16 v[64:79], v[152:155], v[192:195], v[64:79]\n" \
    "s_add_u32 s60, %[wol], 0\n" \
    "s_addc_u32 s61, %[woh], 0\n" \
    "s_add_u32 m0, %[wdst], 85504\n" \
    "s_nop 0\n" \
    "global_load_lds_dwordx4 %[w3off], s[60:61]\n" \
    "s_add_u32 s60, %[wolx], 0\n" \
    "s_addc_u32 s61, %[wohx], 0\n" \
    "s_add_u32 m0, %[wdst], 73216\n" \
    "s_nop 0\n" \
    "global_load_lds_dwordx4 %[w3off], s[60:61]\n" \
    "v_mfma_f32_32x32x16_bf16 v[80:95], v[156:159], v[192:195], v[80:95]\n" \
    "ds_read_b64_tr_b16 v[152:153], v56 offset:54784\n" \
    "ds_read_b64_tr_b16 v[154:155], v56 offset:55040\n" \
    "ds_read_b64_tr_b16 v[156:157], v56 offset:55296\n" \
    "ds_read_b64_tr_b16 v[158:159], v56 offset:55552\n" \
    "s_waitcnt lgkmcnt(8)\n" \
    "v_mfma_f32_32x32x16_bf16 v[64:79], v[136:139], v[196:199], v[64:79]\n" \
    "s_lshr_b32 s60, %[w], 1\n" \
    "s_mul_i32 s60, s60, 6144\n" \
    "v_add_u32 v57, s60, %[rw]\n" \
    "s_add_u32 s60, %[wol], 16384\n" \
    "s_addc_u32 s61, %[woh], 0\n" \
    "s_add_u32 m0, %[wdst], 87552\n" \
    "s_nop 0\n" \
    "global_load_lds_dwordx4 %[w3off], s[60:61]\n" \
    "s_add_u32 s60, %[wolx], 16384\n" \
    "s_addc_u32 s61, %[wohx], 0\n" \
    "s_add_u32 m0, %[wdst], 75264\n" \
    "s_nop 0\n" \
    "global_load_lds_dwordx4 %[w3off], s[60:61]\n" \
    "v_mfma_f32_32x32x16_bf16 v[80:95], v[140:143], v[196:199], v[80:95]\n" \
    "ds_read_b64_tr_b16 v[136:137], %[wa3] offset:33792\n" \
    "ds_read_b64_tr_b16 v[138:139], %[wa3] offset:34048\n" \
    "ds_read_b64_tr_b16 v[140:141], %[wa3] offset:34304\n" \
    "ds_read_b64_tr_b16 v[142:143], %[wa3] offset:34560\n" \
    "s_waitcnt lgkmcnt(8)\n" \
    "v_mfma_f32_32x32x16_bf16 v[64:79], v[144:147], v[200:203], v[64:79]\n" \
    "v_lshlrev_b32 v128, 16, %[xr0]\n" \
    "v_lshlrev_b32 v130, 16, %[xr1]\n" \
    "v_lshlrev_b32 v132, 16, %[xr2]\n" \
    "v_lshlrev_b32 v134, 16, %[xr3]\n" \
    "v_and_b32 v129, 0xffff0000, %[xr0]\n" \
    "v_and_b32 v131, 0xffff0000, %[xr1]\n" \
    "v_and_b32 v133, 0xffff0000, %[xr2]\n" \
    "v_and_b32 v135, 0xffff0000, %[xr3]\n" \
    "v_mfma_f32_32x32x16_bf16 v[80:95], v[148:151], v[200:203], v[80:95]\n" \
    "buffer_load_dword v46, %[oflo], %[rs], 0 offen sc1\n" \
    "ds_read_b64_tr_b16 v[144:145], %[wa3] offset:35840\n" \
    "ds_read_b64_tr_b16 v[146:147], %[wa3] offset:36096\n" \
    "ds_read_b64_tr_b16 v[148:149], %[wa3] offset:36352\n" \
    "ds_read_b64_tr_b16 v[150:151], %[wa3] offset:36608\n" \
    "s_waitcnt lgkmcnt(8)\n" \
    "v_mfma_f32_32x32x16_bf16 v[64:79], v[152:155], v[204:207], v[64:79]\n" \
    "v_lshlrev_b32 v48, 16, %[bq0]\n" \
    "v_lshlrev_b32 v50, 16, %[bq1]\n" \
    "v_lshlrev_b32 v52, 16, %[bq2]\n" \
    "v_lshlrev_b32 v54, 16, %[bq3]\n" \
    "v_and_b32 v49, 0xffff0000, %[bq0]\n" \
    "v_and_b32 v51, 0xffff0000, %[bq1]\n" \
    "v_and_b32 v53, 0xffff0000, %[bq2]\n" \
    "v_and_b32 v55, 0xffff0000, %[bq3]\n" \
    "v_mfma_f32_32x32x16_bf16 v[80:95], v[156:159], v[204:207], v[80:95]\n" \
    "ds_read_b64_tr_b16 v[152:153], %[wa3] offset:37888\n" \
    "ds_read_b64_tr_b16 v[154:155], %[wa3] offset:38144\n" \
    "ds_read_b64_tr_b16 v[156:157], %[wa3] offset:38400\n" \
    "ds_read_b64_tr_b16 v[158:159], %[wa3] offset:38656\n" \
    "s_waitcnt lgkmcnt(8)\n" \
    "v_mfma_f32_32x32x16_bf16 v[64:79], v[136:139], v[208:211], v[64:79]\n" \
    "v_add_f32 v128, v128, v48\n" \
    "v_add_f32 v129, v129, v49\n" \
    "v_add_f32 v130, v130, v50\n" \
    "v_add_f32 v131, v131, v51\n" \
    "v_add_f32 v132, v132, v52\n" \
    "v_add_f32 v133, v133, v53\n" \
    "v_add_f32 v134, v134, v54\n" \
    "v_add_f32 v135, v135, v55\n" \
    "v_mfma_f32_32x32x16_bf16 v[80:95], v[140:143], v[208:211], v[80:95]\n" \
    "s_waitcnt vmcnt(7)\n" \
    "s_barrier\n" \
    "ds_read_b64_tr_b16 v[136:137], %[wa3] offset:0\n" \
    "ds_read_b64_tr_b16 v[138:139], %[wa3] offset:256\n" \
    "ds_read_b64_tr_b16 v[140:141], %[wa3] offset:512\n" \
    "ds_read_b64_tr_b16 v[142:143], %[wa3] offset:768\n" \
    "s_waitcnt lgkmcnt(8)\n" \
    "v_mfma_f32_32x32x16_bf16 v[64:79], v[144:147], v[212:215], v[64:79]\n" \
    "v_mfma_f32_32x32x16_bf16 v[80:95], v[148:151], v[212:215], v[80:95]\n" \
    "ds_read_b64_tr_b16 v[144:145], %[wa3] offset:2048\n" \
    "ds_read_b64_tr_b16 v[146:147], %[wa3] offset:2304\n" \
    "ds_read_b64_tr_b16 v[148:149], %[wa3] offset:2560\n" \
    "ds_read_b64_tr_b16 v[150:151], %[wa3] offset:2816\n" \
    "s_waitcnt lgkmcnt(8)\n" \
    "v_mfma_f32_32x32x16_bf16 v[64:79], v[152:155], v[216:219], v[64:79]\n" \
    "v_mfma_f32_32x32x16_bf16 v[80:95], v[156:159], v[216:219], v[80:95]\n" \
    "s_waitcnt vmcnt(0)\n" \
    "s_mov_b32 s61, 0x40000\n" \
    "4:\n" \
    "v_cmp_ne_u32 vcc, v46, %[tag]\n" \
    "s_nop 1\n" \
    "s_cmp_eq_u64 vcc, 0\n" \
    "s_cbranch_scc1 5f\n" \
    "s_sleep 1\n" \
    "buffer_load_dword v46, %[oflo], %[rs], 0 offen sc1\n" \
    "s_sub_u32 s61, s61, 1\n" \
    "s_waitcnt vmcnt(0)\n" \
    "s_cbranch_scc0 4b\n" \
    "5:\n" \
    "s_movk_i32 s60, 4096\n" \
    "buffer_load_dwordx4 v[96:99], %[ogo], %[rs], 0 offen offset:0 sc1\n" \
    "buffer_load_dwordx4 v[100:103], %[ogo], %[rs], 0 offen offset:1024 sc1\n" \
    "buffer_load_dwordx4 v[104:107], %[ogo], %[rs], 0 offen offset:2048 sc1\n" \
    "buffer_load_dwordx4 v[108:111], %[ogo], %[rs], 0 offen offset:3072 sc1\n" \
    "buffer_load_dwordx4 v[112:115], %[ogo], %[rs], s60 offen offset:0 sc1\n" \
    "buffer_load_dwordx4 v[116:119], %[ogo], %[rs], s60 offen offset:1024 sc1\n" \
    "buffer_load_dwordx4 v[120:123], %[ogo], %[rs], s60 offen offset:2048 sc1\n" \
    "buffer_load_dwordx4 v[124:127], %[ogo], %[rs], s60 offen offset:3072 sc1\n" \
    "ds_read_b64_tr_b16 v[152:153], %[wa3] offset:4096\n" \
    "ds_read_b64_tr_b16 v[154:155], %[wa3] offset:4352\n" \
    "ds_read_b64_tr_b16 v[156:157], %[wa3] offset:4608\n" \
    "ds_read_b64_tr_b16 v[158:159], %[wa3] offset:4864\n" \
    "s_waitcnt lgkmcnt(8)\n" \
    "v_mfma_f32_32x32x16_bf16 v[64:79], v[136:139], v[220:223], v[64:79]\n" \
    "s_add_u32 s60, %[wol], 32768\n" \
    "s_addc_u32 s61, %[woh], 0\n" \
    "s_add_u32 m0, %[wdst], 108032\n" \
    "s_nop 0\n" \
    "global_load_lds_dwordx4 %[w3off], s[60:61]\n" \
    "s_add_u32 s60, %[wolx], 32768\n" \
    "s_addc_u32 s61, %[wohx], 0\n" \
    "s_add_u32 m0, %[wdst], 95744\n" \
    "s_nop 0\n" \
    "global_load_lds_dwordx4 %[w3off], s[60:61]\n" \
    "v_mfma_f32_32x32x16_bf16 v[80:95], v[140:143], v[220:223], v[80:95]\n" \
    "ds_read_b64_tr_b16 v[136:137], %[wa3] offset:58880\n" \
    "ds_read_b64_tr_b16 v[138:139], %[wa3] offset:59136\n" \
    "ds_read_b64_tr_b16 v[140:141], %[wa3] offset:59392\n" \
    "ds_read_b64_tr_b16 v[142:143], %[wa3] offset:59648\n" \
    "s_waitcnt lgkmcnt(8)\n" \
    "v_mfma_f32_32x32x16_bf16 v[64:79], v[144:147], v[224:227], v[64:79]\n" \
    "s_add_u32 s60, %[wol], 49152\n" \
    "s_addc_u32 s61, %[woh], 0\n" \
    "s_add_u32 m0, %[wdst], 110080\n" \
    "s_nop 0\n" \
    "global_load_lds_dwordx4 %[w3off], s[60:61]\n" \
    "s_add_u32 s60, %[wolx], 49152\n" \
    "s_addc_u32 s61, %[wohx], 0\n" \
    "s_add_u32 m0, %[wdst], 97792\n" \
    "s_nop 0\n" \
    "global_load_lds_dwordx4 %[w3off], s[60:61]\n" \
    "v_mfma_f32_32x32x16_bf16 v[80:95], v[148:151], v[224:227], v[80:95]\n" \
    "ds_read_b64_tr_b16 v[144:145], %[wa3] offset:60928\n" \
    "ds_read_b64_tr_b16 v[146:147], %[wa3] offset:61184\n" \
    "ds_read_b64_tr_b16 v[148:149], %[wa3] offset:61440\n" \
    "ds_read_b64_tr_b16 v[150:151], %[wa3] offset:61696\n" \
    "s_waitcnt lgkmcnt(8)\n" \
    "v_mfma_f32_32x32x16_bf16 v[64:79], v[152:155], v[228:231], v[64:79]\n" \
    "s_add_u32 s60, %[wol], 65536\n" \
    "s_addc_u32 s61, %[woh], 0\n" \
    "s_add_u32 m0, %[wdst], 112128\n" \
    "s_nop 0\n" \
    "global_load_lds_dwordx4 %[w3off], s[60:61]\n" \
    "s_add_u32 s60, %[wolx], 65536\n" \
    "s_addc_u32 s61, %[wohx], 0\n" \
    "s_add_u32 m0, %[wdst], 99840\n" \
    "s_nop 0\n" \
    "global_load_lds_dwordx4 %[w3off], s[60:61]\n" \
    "v_mfma_f32_32x32x16_bf16 v[80:95], v[156:159], v[228:231], v[80:95]\n" \
    "ds_read_b64_tr_b16 v[152:153], %[wa3] offset:62976\n" \
    "ds_read_b64_tr_b16 v[154:155], %[wa3] offset:63232\n" \
    "ds_read_b64_tr_b16 v[156:157], %[wa3] offset:63488\n" \
    "ds_read_b64_tr_b16 v[158:159], %[wa3] offset:63744\n" \
    "s_waitcnt lgkmcnt(8)\n" \
    "v_mfma_f32_32x32x16_bf16 v[64:79], v[136:139], v[232:235], v[64:79]\n" \
    "s_add_u32 s60, %[wol], 81920\n" \
    "s_addc_u32 s61, %[woh], 0\n" \
    "s_add_u32 m0, %[wdst], 132608\n" \
    "s_nop 0\n" \
    "global_load_lds_dwordx4 %[w3off], s[60:61]\n" \
    "s_add_u32 s60, %[wolx], 81920\n" \
    "s_addc_u32 s61, %[wohx], 0\n" \
    "s_add_u32 m0, %[wdst], 120320\n" \
    "s_nop 0\n" \
    "global_load_lds_dwordx4 %[w3off], s[60:61]\n" \
    "v_mfma_f32_32x32x16_bf16 v[80:95], v[140:143], v[232:235], v[80:95]\n" \
    "s_waitcnt vmcnt(17)\n" \
    "s_barrier\n" \
    "ds_read_b64_tr_b16 v[136:137], v56 offset:1536\n" \
    "ds_read_b64_tr_b16 v[138:139], v56 offset:1792\n" \
    "ds_read_b64_tr_b16 v[140:141], v56 offset:2048\n" \
    "ds_read_b64_tr_b16 v[142:143], v56 offset:2304\n" \
    "s_waitcnt lgkmcnt(8)\n" \
    "v_mfma_f32_32x32x16_bf16 v[64:79], v[144:147], v[236:239], v[64:79]\n" \
    "s_add_u32 s60, %[wol], 98304\n" \
    "s_addc_u32 s61, %[woh], 0\n" \
    "s_add_u32 m0, %[wdst], 134656\n" \
    "s_nop 0\n" \
    "global_load_lds_dwordx4 %[w3off], s[60:61]\n" \
    "s_add_u32 s60, %[wolx], 98304\n" \
    "s_addc_u32 s61, %[wohx], 0\n" \
    "s_add_u32 m0, %[wdst], 122368\n" \
    "s_nop 0\n" \
    "global_load_lds_dwordx4 %[w3off], s[60:61]\n" \
    "v_mfma_f32_32x32x16_bf16 v[80:95], v[148:151], v[236:239], v[80:95]\n" \
    "ds_read_b64_tr_b16 v[144:145], v56 offset:3584\n" \
    "ds_read_b64_tr_b16 v[146:147], v56 offset:3840\n" \
    "ds_read_b64_tr_b16 v[148:149], v56 offset:4096\n" \
    "ds_read_b64_tr_b16 v[150:151], v56 offset:4352\n" \
    "s_waitcnt lgkmcnt(8)\n" \
    "v_mfma_f32_32x32x16_bf16 v[64:79], v[152:155], v[240:243], v[64:79]\n" \
    "s_add_u32 s60, %[wol], 114688\n" \
    "s_addc_u32 s61, %[woh], 0\n" \
    "s_add_u32 m0, %[wdst], 136704\n" \
    "s_nop 0\n" \
    "global_load_lds_dwordx4 %[w3off], s[60:61]\n" \
    "s_add_u32 s60, %[wolx], 114688\n" \
    "s_addc_u32 s61, %[wohx], 0\n" \
    "s_add_u32 m0, %[wdst], 124416\n" \
    "s_nop 0\n" \
    "global_load_lds_dwordx4 %[w3off], s[60:61]\n" \
    "v_mfma_f32_32x32x16_bf16 v[80:95], v[156:159], v[240:243], v[80:95]\n" \
    "ds_read_b64_tr_b16 v[152:153], v56 offset:5632\n" \
    "ds_read_b64_tr_b16 v[154:155], v56 offset:5888\n" \
    "ds_read_b64_tr_b16 v[156:157], v56 offset:6144\n" \
    "ds_read_b64_tr_b16 v[158:159], v56 offset:6400\n" \
    "s_waitcnt lgkmcnt(8)\n" \
    "v_mfma_f32_32x32x16_bf16 v[64:79], v[136:139], v[244:247], v[64:79]\n" \
    "v_mfma_f32_32x32x16_bf16 v[80:95], v[140:143], v[244:247], v[80:95]\n" \
    "s_waitcnt vmcnt(6)\n" \
    "s_barrier\n" \
    "ds_read_b64_tr_b16 v[136:137], v56 offset:26112\n" \
    "ds_read_b64_tr_b16 v[138:139], v56 offset:26368\n" \
    "ds_read_b64_tr_b16 v[140:141], v56 offset:26624\n" \
    "ds_read_b64_tr_b16 v[142:143], v56 offset:26880\n" \
    "s_waitcnt lgkmcnt(8)\n" \
    "s_waitcnt vmcnt(19)\n" \
    "v_mfma_f32_32x32x16_bf16 v[64:79], v[144:147], v[96:99], v[64:79]\n" \
    "v_mfma_f32_32x32x16_bf16 v[80:95], v[148:151], v[96:99], v[80:95]\n" \
    "ds_read_b64_tr_b16 v[144:145], v56 offset:28160\n" \
    "ds_read_b64_tr_b16 v[146:147], v56 offset:28416\n" \
    "ds_read_b64_tr_b16 v[148:149], v56 offset:28672\n" \
    "ds_read_b64_tr_b16 v[150:151], v56 offset:28928\n" \
    "s_waitcnt lgkmcnt(8)\n" \
    "s_waitcnt vmcnt(18)\n" \
    "v_mfma_f32_32x32x16_bf16 v[64:79], v[152:155], v[100:103], v[64:79]\n" \
    "v_mfma_f32_32x32x16_bf16 v[80:95], v[156:159], v[100:103], v[80:95]\n" \
    "ds_read_b64_tr_b16 v[152:153], v56 offset:30208\n" \
    "ds_read_b64_tr_b16 v[154:155], v56 offset:30464\n" \
    "ds_read_b64_tr_b16 v[156:157], v56 offset:30720\n" \
    "ds_read_b64_tr_b16 v[158:159], v56 offset:30976\n" \
    "s_waitcnt lgkmcnt(8)\n" \
    "s_waitcnt vmcnt(17)\n" \
    "v_mfma_f32_32x32x16_bf16 v[64:79], v[136:139], v[104:107], v[64:79]\n" \
    "v_mfma_f32_32x32x16_bf16 v[80:95], v[140:143], v[104:107], v[80:95]\n" \
    "s_waitcnt vmcnt(0)\n" \
    "s_barrier\n" \
    "ds_read_b64_tr_b16 v[136:137], v56 offset:50688\n" \
    "ds_read_b64_tr_b16 v[138:139], v56 offset:50944\n" \
    "ds_read_b64_tr_b16 v[140:141], v56 offset:51200\n" \
    "ds_read_b64_tr_b16 v[142:143], v56 offset:51456\n" \
    "s_waitcnt lgkmcnt(8)\n" \
    "s_waitcnt vmcnt(16)\n" \
    "v_mfma_f32_32x32x16_bf16 v[64:79], v[144:147], v[108:111], v[64:79]\n" \
    "v_mfma_f32_32x32x16_bf16 v[80:95], v[148:151], v[108:111], v[80:95]\n" \
    "ds_read_b64_tr_b16 v[144:145], v56 offset:52736\n" \
    "ds_read_b64_tr_b16 v[146:147], v56 offset:52992\n" \
    "ds_read_b64_tr_b16 v[148:149], v56 offset:53248\n" \
    "ds_read_b64_tr_b16 v[150:151], v56 offset:53504\n" \
    "s_waitcnt lgkmcnt(8)\n" \
    "s_waitcnt vmcnt(15)\n" \
    "v_mfma_f32_32x32x16_bf16 v[64:79], v[152:155], v[112:115], v[64:79]\n" \
    "v_mfma_f32_32x32x16_bf16 v[80:95], v[156:159], v[112:115], v[80:95]\n" \
    "ds_read_b64_tr_b16 v[152:153], v56 offset:54784\n" \
    "ds_read_b64_tr_b16 v[154:155], v56 offset:55040\n" \
    "ds_read_b64_tr_b16 v[156:157], v56 offset:55296\n" \
    "ds_read_b64_tr_b16 v[158:159], v56 offset:55552\n" \
    "s_waitcnt lgkmcnt(8)\n" \
    "s_waitcnt vmcnt(14)\n" \
    "v_mfma_f32_32x32x16_bf16 v[64:79], v[136:139], v[116:119], v[64:79]\n" \
    "v_mfma_f32_32x32x16_bf16 v[80:95], v[140:143], v[116:119], v[80:95]\n" \
    "s_waitcnt lgkmcnt(4)\n" \
    "s_waitcnt vmcnt(13)\n" \
    "v_mfma_f32_32x32x16_bf16 v[64:79], v[144:147], v[120:123], v[64:79]\n" \
    "v_mfma_f32_32x32x16_bf16 v[80:95], v[148:151], v[120:123], v[80:95]\n" \
    "s_waitcnt lgkmcnt(0)\n" \
    "s_waitcnt vmcnt(12)\n" \
    "v_mfma_f32_32x32x16_bf16 v[64:79], v[152:155], v[124:127], v[64:79]\n" \
    "v_mfma_f32_32x32x16_bf16 v[80:95], v[156:159], v[124:127], v[80:95]\n" \
    "s_bitcmp1_b32 %[w], 1\n" \
    "s_barrier\n" \
    "s_nop 7\n" \
    "s_cbranch_scc1 8f\n" \
    "ds_write_b128 %[rw], v[64:67] offset:0\n" \
    "ds_write_b128 %[rw], v[68:71] offset:1024\n" \
    "ds_write_b128 %[rw], v[72:75] offset:8192\n" \
    "ds_write_b128 %[rw], v[76:79] offset:9216\n" \
    "ds_write_b128 %[rw], v[88:91] offset:24576\n" \
    "ds_write_b128 %[rw], v[92:95] offset:25600\n" \
    "v_add_f32 v128, v128, v80\n" \
    "v_add_f32 v129, v129, v81\n" \
    "v_add_f32 v130, v130, v82\n" \
    "v_add_f32 v131, v131, v83\n" \
    "v_add_f32 v132, v132, v84\n" \
    "v_add_f32 v133, v133, v85\n" \
    "v_add_f32 v134, v134, v86\n" \
    "v_add_f32 v135, v135, v87\n" \
    "s_waitcnt lgkmcnt(0)\n" \
    "s_barrier\n" \
    "ds_read_b128 v[96:99], v57 offset:0\n" \
    "ds_read_b128 v[100:103], v57 offset:1024\n" \
    "ds_read_b128 v[104:107], v57 offset:2048\n" \
    "ds_read_b128 v[108:111], v57 offset:3072\n" \
    "ds_read_b128 v[112:115], v57 offset:6144\n" \
    "ds_read_b128 v[116:119], v57 offset:7168\n" \
    "s_branch 9f\n" \
    "8:\n" \
    "ds_write_b128 %[rw], v[64:67] offset:0\n" \
    "ds_write_b128 %[rw], v[68:71] offset:1024\n" \
    "ds_write_b128 %[rw], v[72:75] offset:8192\n" \
    "ds_write_b128 %[rw], v[76:79] offset:9216\n" \
    "ds_write_b128 %[rw], v[80:83] offset:16384\n" \
    "ds_write_b128 %[rw], v[84:87] offset:17408\n" \
    "v_add_f32 v128, v128, v88\n" \
    "v_add_f32 v129, v129, v89\n" \
    "v_add_f32 v130, v130, v90\n" \
    "v_add_f32 v131, v131, v91\n" \
    "v_add_f32 v132, v132, v92\n" \
    "v_add_f32 v133, v133, v93\n" \
    "v_add_f32 v134, v134, v94\n" \
    "v_add_f32 v135, v135, v95\n" \
    "s_waitcnt lgkmcnt(0)\n" \
    "s_barrier\n" \
    "ds_read_b128 v[96:99], v57 offset:0\n" \
    "ds_read_b128 v[100:103], v57 offset:1024\n" \
    "ds_read_b128 v[104:107], v57 offset:2048\n" \
    "ds_read_b128 v[108:111], v57 offset:3072\n" \
    "ds_read_b128 v[112:115], v57 offset:4096\n" \
    "ds_read_b128 v[116:119], v57 offset:5120\n" \
    "9:\n" \
    "s_waitcnt lgkmcnt(4)\n" \
    "v_add_f32 v128, v128, v96\n" \
    "v_add_f32 v129, v129, v97\n" \
    "v_add_f32 v130, v130, v98\n" \
    "v_add_f32 v131, v131, v99\n" \
    "v_add_f32 v132, v132, v100\n" \
    "v_add_f32 v133, v133, v101\n" \
    "v_add_f32 v134, v134, v102\n" \
    "v_add_f32 v135, v135, v103\n" \
    "s_waitcnt lgkmcnt(0)\n" \
    "v_add_f32 v104, v104, v112\n" \
    "v_add_f32 v105, v105, v113\n" \
    "v_add_f32 v106, v106, v114\n" \
    "v_add_f32 v107, v107, v115\n" \
    "v_add_f32 v108, v108, v116\n" \
    "v_add_f32 v109, v109, v117\n" \
    "v_add_f32 v110, v110, v118\n" \
    "v_add_f32 v111, v111, v119\n" \
    "v_add_f32 v96, v128, v104\n" \
    "v_add_f32 v97, v129, v105\n" \
    "v_add_f32 v98, v130, v106\n" \
    "v_add_f32 v99, v131, v107\n" \
    "v_add_f32 v100, v132, v108\n" \
    "v_add_f32 v101, v133, v109\n" \
    "v_add_f32 v102, v134, v110\n" \
    "v_add_f32 v103, v135, v111\n" \
    "v_cvt_pk_bf16_f32 v104, v96, v97\n" \
    "v_cvt_pk_bf16_f32 v105, v98, v99\n" \
    "v_cvt_pk_bf16_f32 v106, v100, v101\n" \
    "v_cvt_pk_bf16_f32 v107, v102, v103\n" \
    "s_nop 1\n" \
    "v_permlane32_swap_b32 v104, v106\n" \
    "v_permlane32_swap_b32 v105, v107\n" \
    "s_nop 0\n" \
    "global_store_dwordx4 %[oo], v[104:107], %[out]\n" \
    ".Lend_b:\n"
#define P3_CLOB "v46", "v48", "v49", "v50", "v51", "v52", "v53", "v54", "v55", "v56", "v57", "v96", "v97", "v98", "v99", "v100", "v101", "v102", "v103", "v104", "v105", "v106", "v107", "v108", "v109", "v110", "v111", "v112", "v113", "v114", "v115", "v116", "v117", "v118", "v119", "v120", "v121", "v122", "v123", "v124", "v125", "v126", "v127", "v128", "v129", "v130", "v131", "v132", "v133", "v134", "v135", "v136", "v137", "v138", "v139", "v140", "v141", "v142", "v143", "v144", "v145", "v146", "v147", "v148", "v149", "v150", "v151", "v152", "v153", "v154", "v155", "v156", "v157", "v158", "v159"

typedef __attribute__((ext_vector_type(16))) unsigned u32x16;
typedef __attribute__((ext_vector_type(8))) unsigned u32x8;

extern "C" __global__ __launch_bounds__(512, 2) void fused_kernel(const uint16_t* __restrict__ x, const uint16_t* __restrict__ wq, const uint16_t* __restrict__ wk,
                                                       const uint16_t* __restrict__ wv, const uint16_t* __restrict__ wo, const uint16_t* __restrict__ w1,
                                                       const uint16_t* __restrict__ b1, const uint16_t* __restrict__ w2, const uint16_t* __restrict__ b2,
                                                       uint16_t* __restrict__ out, char* ws) {
    extern __shared__ __attribute__((aligned(16))) char lds_raw[];
    const unsigned L = (unsigned)(size_t)(LAS char*)lds_raw;
    unsigned long long xp;
    asm volatile("s_load_dwordx2 %0, %1, 0x0" : "=s"(xp) : "s"(__builtin_amdgcn_kernarg_segment_ptr()));
    __builtin_amdgcn_sched_barrier(0);
    const int tid = threadIdx.x, lane = tid & 63, w = __builtin_amdgcn_readfirstlane(tid >> 6);
    const int b = blockIdx.x, p = ((b & 7) >> 1) * 8 + (b >> 5), g = (b & 1) * 4 + ((b >> 3) & 3);
    const int l31 = lane & 31, h = lane >> 5, l15 = lane & 15, q4 = lane >> 4;
    const unsigned l16 = lane * 16;
    const __amdgpu_buffer_rsrc_t rs = __builtin_amdgcn_make_buffer_rsrc(ws, 0, WS_BYTES, 0x00020000);
    const int nft = 5 + (g < 3), ft0 = 5 * g + min(g, 3);
    const int ft = ft0 + w - 2;
    const bool act = w < 2 + nft;
    const int th3 = w & 1, kq = w >> 1;
    unsigned ep, b1raw; u32x2 b2a, b2b;
    const int rot = 2 * (p & 7) + ((p >> 3) & 1);
    unsigned xo = ((lane + 4 * rot) & 63) * 16 + (p * 64 + w * 8) * 1024;
    const unsigned xd = L + L_X + w * 8 * XP;
    asm volatile("s_waitcnt lgkmcnt(0)" : "+s"(xp), "+v"(xo));
    const char* xs = (const char*)xp;
    __builtin_amdgcn_sched_barrier(0);
#define XROW(i) glds(xo, xs + (i) * 1024, xd + (i) * XP)
    XROW(0);
    const char* ks = (const char*)((g & 1) ? wv : wk);
    glds(l16, ks + ((2 * w + rot) & 15) * 1024, L + L_WKV + w * 2048);
    {
        const unsigned b1o = (unsigned)((w >= 2 && act) ? ft * 32 + l31 : l31) * 2u, b2o = (unsigned)(g * 64 + kq * 16 + h * 4) * 2u;
        asm volatile("global_load_dword %0, %4, %5 sc1\n\tglobal_load_ushort %1, %6, %7\n\tglobal_load_dwordx2 %2, %8, %9\n\tglobal_load_dwordx2 %3, %8, %9 offset:16"
                     : "=&v"(ep), "=&v"(b1raw), "=&v"(b2a), "=&v"(b2b)
                     : "v"(0u), "s"(ws), "v"(b1o), "s"(b1), "v"(b2o), "s"(b2) : "memory");
    }
    XROW(1);
    const unsigned wrow = (w & 3) * 8 + ((lane >> 2) & 7);
    unsigned voA = wrow * 1024u + g * 128 + h * 64 + (lane & 3) * 16;
    unsigned voB = wrow * 2752u + (ft0 + h) * 64 + (lane & 3) * 16;
    unsigned voD = wrow * 2752u + (ft0 + 4 + (h & (nft - 5))) * 64 + (lane & 3) * 16;
    const unsigned wring = L + L_RING + (w & 3) * 4096;
    PIN(voA); PIN(voB); PIN(voD);
    glds(l16, ks + ((2 * w + 1 + rot) & 15) * 1024, L + L_WKV + w * 2048 + 1024);
    const int tg = g >> 1, tkv = tg >> 1;
    const unsigned tile = 2 * p + tkv, tro = (l15 >> 2) * 64 + (q4 & 1) * 32 + (lane & 3) * 8;
    unsigned r_xa, r_a, r_sta, r_so, r_tw, r_tr;
    if (w == 0) {
        r_xa = L + L_X + (16 * tg + l15) * XP + q4 * 16; r_a = L + L_WKV + (q4 * 8 + (l15 >> 2)) * 32 + (lane & 3) * 8;
        r_sta = 0;
        r_so = (g & 1) ? WS_V + tile * 1024 + (tg & 1) * 512 + (q4 >> 1) * 256 + l15 * 16 + (q4 & 1) * 8 : WS_K + tile * 1024 + (q4 & 1) * 512 + ((tg & 1) * 16 + l15) * 16 + (q4 >> 1) * 8;
        r_tw = L + L_SCR + 4096 + l15 * 32 + q4 * 8; r_tr = L + L_SCR + 4096 + (4 * q4 + (l15 >> 2)) * 32 + (lane & 3) * 8;
    } else {
        const int tl = w >> 2, kr = w & 3;
        r_xa = L + L_X + (32 * tl + l31) * XP + h * 16 + kr * 256; r_a = L + L_ST + tl * 2048 + l31 * 64 + (kr * 2 + h) * 8;
        r_sta = L + L_RING + h * 4096 + (w & 1) * 2048 + tro; r_so = WS_CS + (unsigned)(g * 8 + (w & 1) * 4 + h) * 4096u + (p >> 1) * 512 + l31 * 16 + (p & 1) * 8; r_tw = r_tr = 0;
    }
    PIN(r_xa); PIN(r_a); PIN(r_sta); PIN(r_so); PIN(r_tw); PIN(r_tr);
    XROW(2);
    if (tid == 0) lw32(L + L_MISC, 0u);
    unsigned a_cnt = L + L_MISC;
    PIN(a_cnt);
    unsigned a_sta = L + L_ST + lane * 64, a_fxe = L + L_XE + h * 1024 + l31 * 16, a_fxs = L + L_XE + 2048 + lane * 8;
    unsigned a_xa = L + L_X + l31 * XP + h * 16, a_wa = L + L_RING + h * 4096 + w * 512 + tro;
    unsigned a_cso = WS_CS + (unsigned)(g * 8 + w) * 4096u + l16;
    unsigned a_flo = WS_KVF + l16;
    PIN(a_sta); PIN(a_fxe); PIN(a_fxs); PIN(a_xa); PIN(a_wa); PIN(a_cso); PIN(a_flo);
    XROW(3);
    unsigned a_xe = L + L_XE + l16, a_xs = L + L_XE + 2048 + l31 * 8, a_xr = L + L_X + (32 * th3 + l31) * XP + (((g * 64 + kq * 16 + h * 4) * 2 - rot * 64) & 1023);
    unsigned a_ep = w < 2 ? L + L_Q + w * 4096 + l16 : WS_H + (unsigned)(p * 172 + 2 * ft) * 1024u + l16;
    unsigned kvg = WS_K + (unsigned)((w >> 2) * 32 + (w & 3)) * 1024u + l16;
    const int tq = w & 3, kh = w >> 2;
    const int ks0 = kq < 2 ? 22 * kq : 21 * kq + 2;
    const unsigned long long w2b = (unsigned long long)w2 + (unsigned)ks0 * 16384u, wob = (unsigned long long)wo + (unsigned)kq * 131072u;
    PIN(a_xe); PIN(a_xs); PIN(a_xr); PIN(a_ep); PIN(kvg);
    XROW(4);
    unsigned t_kw = L + w * 1024 + l16, t_vw = L + 8192 + w * VT + (VS - 512) + h * VS + l31 * 16, t_cw = L + 8192 + w * VT + ((lane >> 1) & 1) * VS + (lane & 1) * 16;
    unsigned t_ka = L + kh * 4096 + l16, t_va = L + 8192 + kh * 4 * VT + (l31 < 16 ? (VS - 512) + h * 256 + l31 * 16 : ((l31 & 27) == 16 ? 0 : 16));
    unsigned t_qa = L + L_Q + ((tq >> 1) * 2 + (tq & 1)) * 2048 + l16;
    PIN(t_kw); PIN(t_vw); PIN(t_cw); PIN(t_ka); PIN(t_va); PIN(t_qa);
    XROW(5);
    unsigned t_hflo = WS_HF + p * 256 + lane * 4, t_hgo = WS_H + (unsigned)((p * 2 + th3) * 86 + ks0) * 1024u + l16;
    unsigned w3off = (unsigned)(((q4 & 1) * 8 + 4 * th3 + ((lane >> 2) & 3)) * 1024 + g * 128 + h * 64 + (lane & 3) * 16);
    unsigned t_mw = L + w * 2048 + l16, t_lw = L + L_ST + 512 + w * 256 + lane * 4;
    PIN(t_hflo); PIN(t_hgo); PIN(w3off); PIN(t_mw); PIN(t_lw);
    XROW(6);
    const unsigned ohd = (tq & 1) * 32 + 4 * g + (tq >> 1) * 2 + kh;
    unsigned f_oso = WS_O + (unsigned)(p * 64 + ohd) * 1024u + l16, f_oflo = WS_OF + (p * 8 + 2 * kq + (lane & 1)) * 128;
    unsigned f_ogo = WS_O + (unsigned)((p * 2 + th3) * 32 + 8 * kq) * 1024u + l16, f_wa3 = L + kq * 6144 + h * 1024 + tro;
    unsigned f_rw = L + th3 * 32768 + kq * 2048 + l16, f_oo = (unsigned)((p * 64 + th3 * 32 + l31) * 512 + g * 64 + kq * 16 + h * 8) * 2u;
    PIN(f_oso); PIN(f_oflo); PIN(f_ogo); PIN(f_wa3); PIN(f_rw); PIN(f_oo);
    XROW(7);
    const int sh16 = (p & 7) * 2 + th3;
    const char* pfp = (lane < 10 ? (const char*)w2b + min(192 + sh16 * 10 + lane, kq < 2 ? 351 : 335) * 1024 : (const char*)wob + (sh16 * 8 + (lane & 7)) * 1024) + g * 128;
    PIN(pfp);
    if (w >= 4) static_for<PRE>([&](auto I) {
        const char* s1 = (const char*)w1 + ((I + rot) & 15) * 32 * 2752;
        glds(voA, (const char*)wq + ((I + rot) & 15) * 32 * 1024, wring + I * 16384);
        glds(voB, s1, wring + I * 16384 + 1024); glds(voB, s1 + 128, wring + I * 16384 + 2048); glds(voD, s1, wring + I * 16384 + 3072);
    });
    voA += ((PRE + rot) & 15) * 32 * 1024; voB += ((PRE + rot) & 15) * 32 * 2752; voD += ((PRE + rot) & 15) * 32 * 2752;
    if (w < 4) {
        unsigned long long pc; unsigned csz;
        asm volatile("s_getpc_b64 s[62:63]\n.Lpc_here:\n\ts_sub_u32 s62, s62, .Lpc_here-fused_kernel\n\ts_subb_u32 s63, s63, 0\n\ts_mov_b64 %0, s[62:63]\n\ts_mov_b32 %1, .Lend_a-fused_kernel-4\n\ts_max_u32 %1, %1, .Lend_b-fused_kernel-4"
                     : "=s"(pc), "=s"(csz)::"scc", "s62", "s63");
        touch(min((unsigned)(w * 8192 + lane * 128), csz), (const char*)pc, L + L_SCR + w * 256);
        waitv<1>();
    } else waitv<4 * PRE>();
    asm volatile("s_waitcnt lgkmcnt(0)\n\ts_barrier" : "+v"(ep), "+v"(b1raw), "+v"(b2a), "+v"(b2b)::"memory");
    const unsigned tag = ep + 1u + (unsigned)dispatch_id() * 0x9E3779B1u;
    if (w == 0) {
        asm volatile(KV_ASM :: [m1] "s"(0xAAAAAAAAAAAAAAAAull), [m2] "s"(0xCCCCCCCCCCCCCCCCull), [mq] "s"(0xF0000F0000F0000Full), [xa] "v"(r_xa), [wa] "v"(r_a), [so] "v"(r_so), [tw] "v"(r_tw), [tr] "v"(r_tr), [isv] "s"(g & 1), [rs] "s"(rs)
                     : "memory", "scc", "s60", "s61", KV_CLOB);
    } else {
        if (w >= 4) asm volatile(PRE_ASM : [voA] "+v"(voA), [voB] "+v"(voB), [voD] "+v"(voD)
                                 : [cnt] "v"(a_cnt), [srcA] "s"(wq), [srcB] "s"(w1), [ring] "s"(wring), [stepA] "s"(32 * 1024u), [stepB] "s"(32 * 2752u), [totA] "s"(512 * 1024u), [totB] "s"(512 * 2752u)
                                 : "memory", "scc", "v159");
        asm volatile(ST_ASM :: [xa] "v"(r_xa), [sw] "v"(r_a), [m1] "s"(0xAAAAAAAAAAAAAAAAull), [m2] "s"(0xCCCCCCCCCCCCCCCCull) : "memory", ST_CLOB);
        if (w == 1) asm volatile(ST_ASM :: [xa] "v"(r_xa - 256), [sw] "v"(r_a - 16), [m1] "s"(0xAAAAAAAAAAAAAAAAull), [m2] "s"(0xCCCCCCCCCCCCCCCCull) : "memory", ST_CLOB);
        if ((w == 2 || w == 3) && p < 16) asm volatile(CSP_ASM ::[cnt] "v"(a_cnt), [wa] "v"(r_sta), [csp] "v"(r_so), [tag] "v"(tag), [one] "s"(0x3F803F80u), [rs] "s"(rs) : "memory", "scc", "s60", "s61", ST_CLOB);
    }
    const unsigned bb = (w >= 2) ? (b1raw | (b1raw << 16)) : 0u;
    f32x16 acc0, acc1;
    f32x4 rsg;
    u32x16 kva; u32x8 kvb;
    u32x2 xr0, xr1;
    unsigned long long kvbad;
    {
        u32x8 xe; unsigned long long csbad = 0;
        if (w < 4) {
            asm volatile(P1L_ASM : "=&{v[64:79]}"(acc0), "=&{v[80:95]}"(acc1), "=&{v[144:151]}"(xe), "=&{v[152:155]}"(rsg), "=&{v[196:211]}"(kva), "=&{v[212:219]}"(kvb),
                           [xr0] "=&v"(xr0), [xr1] "=&v"(xr1), [csbad] "=&s"(csbad), [kvbad] "=&s"(kvbad)
                         : [cnt] "v"(a_cnt), [cso] "v"(a_cso), [xa] "v"(a_xa), [wa] "v"(a_wa), [w] "s"(w), [tag] "v"(tag), [bb] "v"(bb), [fo] "s"(WS_KVF + b * 4), [rs] "s"(rs),
                            [flo] "v"(a_flo), [kvg] "v"(kvg), [xe] "v"(a_xe), [xs] "v"(a_xs), [xr] "v"(a_xr),
                           [sta] "v"(a_sta), [fxe] "v"(a_fxe), [fxs] "v"(a_fxs)
                         : "memory", "scc", "vcc", "s60", "s61", P1_CLOB);
        } else if (act) {
            asm volatile(P1H_ASM : "=&{v[64:79]}"(acc0), "=&{v[80:95]}"(acc1), "=&{v[144:151]}"(xe), "=&{v[152:155]}"(rsg), "=&{v[196:211]}"(kva), "=&{v[212:219]}"(kvb),
                           [xr0] "=&v"(xr0), [xr1] "=&v"(xr1), [voA] "+v"(voA), [voB] "+v"(voB), [voD] "+v"(voD), [xa] "+v"(a_xa), [csbad] "=&s"(csbad), [kvbad] "=&s"(kvbad)
                         : [cnt] "v"(a_cnt), [cso] "v"(a_cso), [wa] "v"(a_wa), [srcA] "s"(wq), [srcB] "s"(w1), [ring] "s"(wring), [stepA] "s"(32 * 1024u), [stepB] "s"(32 * 2752u), [totA] "s"(512 * 1024u),
                           [totB] "s"(512 * 2752u), [bb] "v"(bb), [rs] "s"(rs), [tag] "v"(tag), [flo] "v"(a_flo), [kvg] "v"(kvg), [xe] "v"(a_xe), [xs] "v"(a_xs), [xr] "v"(a_xr)
                         : "memory", "scc", "vcc", "s60", "s61", P1_CLOB);
        } else {
            asm volatile(P1I_ASM : "=&{v[196:211]}"(kva), "=&{v[212:219]}"(kvb), [xr0] "=&v"(xr0), [xr1] "=&v"(xr1), [voA] "+v"(voA), [voB] "+v"(voB), [voD] "+v"(voD), [kvbad] "=&s"(kvbad)
                         : [srcA] "s"(wq), [srcB] "s"(w1), [ring] "s"(wring), [stepA] "s"(32 * 1024u), [stepB] "s"(32 * 2752u), [totA] "s"(512 * 1024u), [totB] "s"(512 * 2752u),
                           [rs] "s"(rs), [kvg] "v"(kvg), [xr] "v"(a_xr), [tag] "v"(tag), [flo] "v"(a_flo)
                         : "memory", "scc", "vcc", "s60", "s61", P1_CLOB);
        }
        if (__builtin_expect(csbad != 0, 0)) {
            float c;
            for (unsigned it = 0;; ++it) {
                bool ok = true; c = 0.f;
#pragma unroll
                for (int i = 0; i < 4; ++i) { const u32x4 v = LD128(a_cso + i * 1024); ok &= v[1] == tag && v[3] == tag; c += __uint_as_float(v[0]) + __uint_as_float(v[2]); }
                if (__all(ok) || it >= SPIN_CAP) break;
                __builtin_amdgcn_s_sleep(8);
            }
            c = add32(c);
            const u32x4 we = h ? (u32x4){bb, bb & 0xFFFFu, 0u, 0u} : ext_c(c);
            acc0 = mfma32(we, (u32x4){xe[0], xe[1], xe[2], xe[3]}, acc0); acc1 = mfma32(we, (u32x4){xe[4], xe[5], xe[6], xe[7]}, acc1);
        }
    }
    u32x16 hfa;
    if (w < 2) asm volatile(QEPI_ASM : "+{v[64:79]}"(acc0), "+{v[80:95]}"(acc1), "+{v[152:155]}"(rsg) : [qw] "v"(a_ep) : "memory", EPI_CLOB);
    else if (act) asm volatile(GELU_ASM : "=&{v[160:175]}"(hfa) : "{v[64:79]}"(acc0), "{v[80:95]}"(acc1), "{v[152:155]}"(rsg) : EPI_CLOB);
    if (__builtin_expect(kvbad != 0, 0)) {
        for (unsigned it = 0;; ++it) {
            const u32x4 fl = LD128(a_flo);
            if (__all(fl[0] == tag && fl[1] == tag && fl[2] == tag && fl[3] == tag) || it >= SPIN_CAP) break;
            __builtin_amdgcn_s_sleep(16);
        }
        CFENCE();
        static_for<3>([&](auto C) {
            const u32x4 k = LD128(kvg + C * 4096), v = LD128(kvg + 65536 + C * 4096);
#pragma unroll
            for (int i = 0; i < 4; ++i) { if constexpr (C < 2) { kva[8 * C + i] = k[i]; kva[8 * C + 4 + i] = v[i]; } else { kvb[i] = k[i]; kvb[4 + i] = v[i]; } }
        });
        asm volatile("s_waitcnt vmcnt(0)" : "+{v[196:211]}"(kva), "+{v[212:219]}"(kvb)::"memory");
    }
    if (b == 0 && tid == 0) ST32(tag, WS_EPOCH);
    f32x16 OA, OB;
    u32x8 q2;
    u32x16 hfb; u32x4 hf0; u32x16 hfc; u32x8 hfd; u32x4 hfe;
    unsigned long long bad;
    const unsigned wdst = L + kq * 6144 + th3 * 1024;
    {
        asm volatile(ATT_ASM : "=&{v[64:79]}"(OA), "=&{v[80:95]}"(OB), "=&{v[128:135]}"(q2), "+{v[160:175]}"(hfa), "=&{v[176:191]}"(hfb), "=&{v[192:195]}"(hf0), "+{v[196:211]}"(kva), "+{v[212:219]}"(kvb),
                       "=&{v[220:223]}"(hfe), "=&{v[224:239]}"(hfc), "=&{v[240:247]}"(hfd), [bad] "=&s"(bad)
                     : [ka] "v"(t_ka), [va] "v"(t_va), [kw] "v"(t_kw), [vw] "v"(t_vw), [cw] "v"(t_cw), [qa] "v"(t_qa), [kvg] "v"(kvg), [rs] "s"(rs),
                       [w2l] "s"((unsigned)w2b), [w2h] "s"((unsigned)(w2b >> 32)), [wdst] "s"(wdst), [w3off] "v"(w3off), [pfp] "v"(pfp), [scr] "s"(L + L_XE), [hflo] "v"(t_hflo),
                       [hfo] "s"(WS_HF + p * 256 + ft * 4), [nft] "s"(nft), [tag] "v"(tag), [hgo] "v"(t_hgo), [h21] "s"(kq < 2 ? 21u * 1024u : WS_BYTES), [hmask] "s"((1ull << 43) - 1),
                       [m1] "s"(0xAAAAAAAAAAAAAAAAull), [w] "s"(w), [mw] "v"(t_mw), [lw] "v"(t_lw), [oso] "v"(f_oso), [hso] "v"(a_ep)
                     : "memory", "scc", "vcc", "s60", "s61", ATT_CLOB);
    }
    if (__builtin_expect(bad != 0, 0)) {
        const u32x4 ONES = {0x3F803F80u, 0x3F803F80u, 0x3F803F80u, 0x3F803F80u};
        const unsigned ln = (f_oso >> 4) & 63;
        const u32x4 q = kh ? (u32x4){q2[4], q2[5], q2[6], q2[7]} : (u32x4){q2[0], q2[1], q2[2], q2[3]};
        f32x16 O = {}; float m = -INFINITY;
#pragma nounroll
        for (int t = 0; t < 64; ++t) {
            const u32x4 kf = LD128(WS_K + t * 1024 + ln * 16);
            u32x4 v0 = LD128(WS_V + t * 1024 + (ln >> 5) * 256 + (ln & 15) * 16), v1 = LD128(WS_V + t * 1024 + 512 + (ln >> 5) * 256 + (ln & 15) * 16);
            if (ln & 16) { v0 = ONES; v1 = ONES; }
            f32x16 s = mfma32(kf, q, (f32x16){});
            float mx = s[0];
#pragma unroll
            for (int i = 1; i < 16; ++i) mx = fmaxf(mx, s[i]);
            const float mn = fmaxf(m, max32(mx)), al = __builtin_amdgcn_exp2f(m - mn); m = mn;
#pragma unroll
            for (int i = 0; i < 16; ++i) { O[i] *= al; s[i] = __builtin_amdgcn_exp2f(s[i] - mn); }
            O = smf(v0, __builtin_shufflevector(s, s, 0, 1, 2, 3, 4, 5, 6, 7), O, 0xDDDDDDDDu);
            O = smf(v1, __builtin_shufflevector(s, s, 8, 9, 10, 11, 12, 13, 14, 15), O, 0xDDDDDDDDu);
        }
        const float inv = __builtin_amdgcn_rcpf(O[8]);
        waitv<0>();
        ST128(((u32x4){cvtpk(O[0] * inv, O[1] * inv), cvtpk(O[2] * inv, O[3] * inv), cvtpk(O[4] * inv, O[5] * inv), cvtpk(O[6] * inv, O[7] * inv)}), f_oso);
        CFENCE();
    }
    {
        if (w < 4) {
            asm volatile(P3L_ASM : "=&{v[64:79]}"(OA), "=&{v[80:95]}"(OB)
                         : "{v[160:175]}"(hfa), "{v[176:191]}"(hfb), "{v[192:195]}"(hf0), "{v[196:211]}"(kva), "{v[212:219]}"(kvb), "{v[220:223]}"(hfe), "{v[224:239]}"(hfc), "{v[240:247]}"(hfd),
                           [wa3] "v"(f_wa3), [rs] "s"(rs), [w] "s"(w), [tag] "v"(tag), [ofo] "s"(WS_OF + (p * 8 + g) * 128), [oflo] "v"(f_oflo), [ogo] "v"(f_ogo),
                           [xr0] "v"(xr0[0]), [xr1] "v"(xr0[1]), [xr2] "v"(xr1[0]), [xr3] "v"(xr1[1]), [bq0] "v"(b2a[0]), [bq1] "v"(b2a[1]), [bq2] "v"(b2b[0]), [bq3] "v"(b2b[1]),
                           [rw] "v"(f_rw), [oo] "v"(f_oo), [out] "s"(out)
                         : "memory", "scc", "vcc", "s60", "s61", P3_CLOB);
        } else {
            const unsigned long long w2x = (unsigned long long)w2 + (unsigned)(kq - 2) * 22u * 16384u, wox = wob - 262144;
            asm volatile(P3H_ASM : "=&{v[64:79]}"(OA), "=&{v[80:95]}"(OB)
                         : "{v[160:175]}"(hfa), "{v[176:191]}"(hfb), "{v[192:195]}"(hf0), "{v[196:211]}"(kva), "{v[212:219]}"(kvb), "{v[220:223]}"(hfe), "{v[224:239]}"(hfc), "{v[240:247]}"(hfd),
                           [wa3] "v"(f_wa3), [rs] "s"(rs), [w] "s"(w), [tag] "v"(tag), [oflo] "v"(f_oflo), [ogo] "v"(f_ogo), [wdst] "s"(wdst), [w3off] "v"(w3off),
                           [w2l] "s"((unsigned)w2b), [w2h] "s"((unsigned)(w2b >> 32)), [wol] "s"((unsigned)wob), [woh] "s"((unsigned)(wob >> 32)),
                           [w2lx] "s"((unsigned)w2x), [w2hx] "s"((unsigned)(w2x >> 32)), [wolx] "s"((unsigned)wox), [wohx] "s"((unsigned)(wox >> 32)),
                           [xr0] "v"(xr0[0]), [xr1] "v"(xr0[1]), [xr2] "v"(xr1[0]), [xr3] "v"(xr1[1]), [bq0] "v"(b2a[0]), [bq1] "v"(b2a[1]), [bq2] "v"(b2b[0]), [bq3] "v"(b2b[1]),
                           [rw] "v"(f_rw), [oo] "v"(f_oo), [out] "s"(out)
                         : "memory", "scc", "vcc", "s60", "s61", P3_CLOB);
        }
    }
}

extern "C" void kernel_launch(void* const* d_in, const int* in_sizes, int n_in, void* d_out, int out_size, void* d_ws, size_t ws_size, hipStream_t stream) {
    (void)in_sizes; (void)n_in; (void)out_size; (void)ws_size;
    (void)hipFuncSetAttribute((const void*)fused_kernel, hipFuncAttributeMaxDynamicSharedMemorySize, LDS_BYTES);
    fused_kernel<<<256, 512, LDS_BYTES, stream>>>((const uint16_t*)d_in[0], (const uint16_t*)d_in[1], (const uint16_t*)d_in[2], (const uint16_t*)d_in[3],
                                                  (const uint16_t*)d_in[4], (const uint16_t*)d_in[6], (const uint16_t*)d_in[7], (const uint16_t*)d_in[8],
                                                  (const uint16_t*)d_in[9], (uint16_t*)d_out, (char*)d_ws);
}
```
